```python
import torch

def _softmax(x, axis=-1):
    return torch.softmax(x, dim=axis)
def _gelu(x):
    return torch.nn.functional.gelu(x, approximate="tanh")
def _silu(x):
    return torch.nn.functional.silu(x)
def _sigmoid(x):
    return torch.sigmoid(x)
def _layernorm(x, axis=-1, eps=1e-5):
    m = x.mean(dim=axis, keepdim=True)
    v = x.var(dim=axis, keepdim=True, unbiased=False)
    return (x - m) / torch.sqrt(v + eps)
def _scatter_add(tgt, idx, src, axis=0):
    return tgt.clone().index_add_(axis, idx, src)
def _segment_sum(src, seg, n):
    out = torch.zeros((n,) + src.shape[1:], dtype=src.dtype)
    return out.index_add_(0, seg, src)
def _pad(x, pw):
    flat = tuple(v for pair in reversed(pw) for v in pair)
    return torch.nn.functional.pad(x, flat)

kernel_name = 'parallel_sum__ssm_conv_h3__moe_arch_mod__prompt'

def setup_inputs():

    _w = (lambda a: (lambda t: t.to(torch.bfloat16) if t.is_floating_point() else t)(torch.as_tensor(a)))
    def _A_setup():
        B, S, D = 1, 2048, 1024
        ks = 4
        return {
            "q": _w((lambda *s: torch.randn(*s, dtype=torch.bfloat16) * 0.05)(B, S, D)),
            "k": _w((lambda *s: torch.randn(*s, dtype=torch.bfloat16) * 0.8)(B, S, D)),
            "v": _w((lambda *s: torch.randn(*s, dtype=torch.bfloat16) * 0.8)(B, S, D)),
            "shift_k": _w((lambda *s: torch.randn(*s, dtype=torch.bfloat16) * 0.8)(D, ks) * 0.5),
            "ssm_k": _w((lambda *s: torch.randn(*s, dtype=torch.bfloat16) * 0.8)(D, S) * (1.0 / S ** 0.5)),
            "ssm_D": _w((lambda *s: torch.randn(*s, dtype=torch.bfloat16) * 0.8)(D)),
        }
    _A_in = _A_setup()

    def _B_setup():
        B, S, D, F, C = 1, 2048, 1024, 3584, 2
        return {
            "x": _w((lambda *s: torch.randn(*s, dtype=torch.bfloat16) * 0.05)(B, S, D)),
            "w_r": _w((lambda *s: torch.randn(*s, dtype=torch.bfloat16) * 0.05)(D)),
            "w_gate": _w((lambda *s: torch.randn(*s, dtype=torch.bfloat16) * 0.05)(D, F)),
            "w_up": _w((lambda *s: torch.randn(*s, dtype=torch.bfloat16) * 0.05)(D, F)),
            "w_down": _w((lambda *s: torch.randn(*s, dtype=torch.bfloat16) * 0.05)(F, D)),
        }
    _B_in = _B_setup()

    return {
        "A_q": _A_in["q"],
        "A_k": _A_in["k"],
        "A_v": _A_in["v"],
        "A_shift_k": _A_in["shift_k"],
        "A_ssm_k": _A_in["ssm_k"],
        "A_ssm_D": _A_in["ssm_D"],
        "B_x": _B_in["x"],
        "B_w_r": _B_in["w_r"],
        "B_w_gate": _B_in["w_gate"],
        "B_w_up": _B_in["w_up"],
        "B_w_down": _B_in["w_down"],
    }

def reference(A_q, A_k, A_v, A_shift_k, A_ssm_k, A_ssm_D, B_x, B_w_r, B_w_gate, B_w_up, B_w_down):
    def _A_ref(q, k, v, shift_k, ssm_k, ssm_D):
        B, S, D = q.shape
        ks = shift_k.shape[-1]
        k_pad = _pad(k, ((0, 0), (ks - 1, 0), (0, 0)))
        k_shift = torch.zeros_like(k)
        for t in range(ks):
            k_shift = k_shift + k_pad[:, t:t + S, :] * shift_k[:, ks - 1 - t][None, None, :]
        kv = k_shift * v
        L = 2 * S
        kv_pad = _pad(kv, ((0, 0), (0, S), (0, 0)))
        kf_pad = _pad(ssm_k, ((0, 0), (0, S)))
        kv_f = (lambda x, axes=(-2,-1): torch.fft.rfft2(x.float(), dim=axes))(kv_pad[:, None, :, :], axes=(1, 2))
        kf_f = (lambda x, axes=(-2,-1): torch.fft.rfft2(x.float(), dim=axes))(kf_pad[:, None, :], axes=(1, 2))
        kf_f = (lambda x, *d: x.permute(*d))(kf_f, 1, 2, 0)
        y_f = kv_f * kf_f[None, :, :, :]
        y = (lambda x, s, axes=(-2,-1): torch.fft.irfft2(x, s=s, dim=axes).to(torch.bfloat16))(y_f, (1, L), axes=(1, 2))[:, 0, :S, :]
        y = y + kv * ssm_D[None, None, :]
        return q * y

    def _B_ref(x, w_r, w_gate, w_up, w_down):
        B, S, D = x.shape
        C = 2
        r = x @ w_r
        idx = torch.argsort(-r, axis=-1)[:, :C]
        idx_e = (lambda x, n, axis: torch.repeat_interleave(x, n, dim=axis))(idx.reshape(B, C, 1), D, axis=2)
        x_sel = (lambda x, i, axis: torch.take_along_dim(x, i, dim=axis))(x, idx_e, axis=1)
        g = _silu(x_sel @ w_gate)
        u = x_sel @ w_up
        y = (g * u) @ w_down
        r_sel = _sigmoid((lambda x, i, axis: torch.take_along_dim(x, i, dim=axis))(r, idx, axis=1))
        y = y * r_sel.reshape(B, C, 1)
        b_off = (torch.arange(B) * S).reshape(B, 1)
        flat_idx = (idx + b_off).reshape(B * C)
        zero = (x * 0.0).reshape(B * S, D)
        upd = _scatter_add(zero, flat_idx, y.reshape(B * C, D), axis=0)
        return x + upd.reshape(B, S, D)

    x = A_q
    xn = _layernorm(x, axis=-1)
    return x + _A_ref(xn, A_k, A_v, A_shift_k, A_ssm_k, A_ssm_D) + _B_ref(xn, B_w_r, B_w_gate, B_w_up, B_w_down)
```

```cpp
#include <hip/hip_runtime.h>
#include <hip/hip_bf16.h>
#include <stdint.h>

typedef short bf16x8 __attribute__((ext_vector_type(8)));
typedef float f32x4 __attribute__((ext_vector_type(4)));
typedef unsigned v4u __attribute__((ext_vector_type(4)));
typedef unsigned short u16;

constexpr int S = 2048, D = 1024, F = 3584;

__device__ __forceinline__ float bf2f(u16 b) { return __builtin_bit_cast(float, (unsigned)b << 16); }
__device__ __forceinline__ u16 f2bf(float f) { return __builtin_bit_cast(u16, (__bf16)f); }
__device__ __forceinline__ float rbf(float f) { return bf2f(f2bf(f)); }

__global__ void __launch_bounds__(256) k1(const u16* q, const u16* k, const u16* v, const u16* shk, const u16* wr,
                                          float* stats, float* rr, u16* kvT) {
  int wave = threadIdx.x >> 6, lane = threadIdx.x & 63;
  for (int rI = 0; rI < 2; ++rI) {
    int row = blockIdx.x * 8 + wave * 2 + rI;
    float x[16];
    float sum = 0.f;
    for (int i = 0; i < 16; ++i) { x[i] = bf2f(q[row * D + i * 64 + lane]); sum += x[i]; }
    for (int o = 32; o; o >>= 1) sum += __shfl_xor(sum, o);
    float mean = sum * (1.f / D);
    float vs = 0.f;
    for (int i = 0; i < 16; ++i) { float d = x[i] - mean; vs += d * d; }
    for (int o = 32; o; o >>= 1) vs += __shfl_xor(vs, o);
    float var = rbf(vs * (1.f / D));
    float m = rbf(mean);
    float sd = rbf(sqrtf(rbf(var + 1e-5f)));
    float r = 0.f;
    for (int i = 0; i < 16; ++i) { float xn = rbf(rbf(x[i] - m) / sd); r += xn * bf2f(wr[i * 64 + lane]); }
    for (int o = 32; o; o >>= 1) r += __shfl_xor(r, o);
    if (lane == 0) { stats[row * 2] = m; stats[row * 2 + 1] = sd; rr[row] = rbf(r); }
    for (int i = 0; i < 16; ++i) {
      int d = i * 64 + lane;
      float ks = 0.f;
      for (int j = 3; j >= 0; --j) {
        float kk = (row - j >= 0) ? bf2f(k[(row - j) * D + d]) : 0.f;
        ks = rbf(ks + rbf(kk * bf2f(shk[d * 4 + j])));
      }
      kvT[d * S + row] = f2bf(ks * bf2f(v[row * D + d]));
    }
  }
}

__device__ void top2(const float* rr, int* sidx, float* sval) {
  __shared__ float bv[2][256]; __shared__ int bi[2][256];
  float v1 = -INFINITY, v2 = -INFINITY; int i1 = 0x7fffffff, i2 = 0x7fffffff;
  for (int i = threadIdx.x; i < S; i += 256) {
    float r = rr[i];
    if (r > v1 || (r == v1 && i < i1)) { v2 = v1; i2 = i1; v1 = r; i1 = i; }
    else if (r > v2 || (r == v2 && i < i2)) { v2 = r; i2 = i; }
  }
  bv[0][threadIdx.x] = v1; bv[1][threadIdx.x] = v2; bi[0][threadIdx.x] = i1; bi[1][threadIdx.x] = i2;
  __syncthreads();
  if (threadIdx.x == 0) {
    v1 = -INFINITY; v2 = -INFINITY; i1 = 0x7fffffff; i2 = 0x7fffffff;
    for (int t = 0; t < 256; ++t) for (int e = 0; e < 2; ++e) {
      float r = bv[e][t]; int i = bi[e][t];
      if (r > v1 || (r == v1 && i < i1)) { v2 = v1; i2 = i1; v1 = r; i1 = i; }
      else if (r > v2 || (r == v2 && i < i2)) { v2 = r; i2 = i; }
    }
    sidx[0] = i1; sidx[1] = i2; sval[0] = v1; sval[1] = v2;
  }
  __syncthreads();
}

__global__ void __launch_bounds__(256) k2(const u16* q, const float* stats, const float* rr, const u16* wg, const u16* wu,
                                          float* hbuf, int* selidx, float* selval) {
  __shared__ int sidx[2]; __shared__ float sval[2];
  __shared__ float xs[2][D];
  __shared__ float red[4][4][64];
  top2(rr, sidx, sval);
  if (blockIdx.x == 0 && threadIdx.x < 2) { selidx[threadIdx.x] = sidx[threadIdx.x]; selval[threadIdx.x] = sval[threadIdx.x]; }
  for (int i = threadIdx.x; i < 2 * D; i += 256) {
    int t = i / D, d = i % D; int row = sidx[t];
    xs[t][d] = rbf(rbf(bf2f(q[row * D + d]) - stats[row * 2]) / stats[row * 2 + 1]);
  }
  __syncthreads();
  int wave = threadIdx.x >> 6, lane = threadIdx.x & 63;
  int f = blockIdx.x * 64 + lane;
  float g0 = 0, g1 = 0, u0 = 0, u1 = 0;
  for (int d = wave * 256; d < wave * 256 + 256; ++d) {
    float a = bf2f(wg[d * F + f]), b = bf2f(wu[d * F + f]);
    g0 += xs[0][d] * a; g1 += xs[1][d] * a; u0 += xs[0][d] * b; u1 += xs[1][d] * b;
  }
  red[wave][0][lane] = g0; red[wave][1][lane] = g1; red[wave][2][lane] = u0; red[wave][3][lane] = u1;
  __syncthreads();
  if (wave < 2) {
    float g = 0, u = 0;
    for (int w = 0; w < 4; ++w) { g += red[w][wave][lane]; u += red[w][2 + wave][lane]; }
    g = rbf(g); u = rbf(u);
    float sg = rbf(g / (1.f + __expf(-g)));
    hbuf[wave * F + f] = rbf(sg * u);
  }
}

__global__ void __launch_bounds__(256) k3(const float* hbuf, const u16* wd, const float* selval, float* upd) {
  __shared__ float red[4][2][64];
  int wave = threadIdx.x >> 6, lane = threadIdx.x & 63;
  int d = blockIdx.x * 64 + lane;
  float y0 = 0, y1 = 0;
  for (int f = wave * (F / 4); f < (wave + 1) * (F / 4); ++f) {
    float w = bf2f(wd[f * D + d]);
    y0 += hbuf[f] * w; y1 += hbuf[F + f] * w;
  }
  red[wave][0][lane] = y0; red[wave][1][lane] = y1;
  __syncthreads();
  if (wave < 2) {
    float y = 0;
    for (int w = 0; w < 4; ++w) y += red[w][wave][lane];
    y = rbf(y);
    float rs = rbf(1.f / (1.f + __expf(-selval[wave])));
    upd[wave * D + d] = rbf(y * rs);
  }
}

__global__ void __launch_bounds__(256) k4(const u16* q, const u16* kvT, const u16* ssmk, const u16* ssmD, const float* stats,
                                          const int* selidx, const float* upd, u16* out) {
  __shared__ __attribute__((aligned(16))) u16 ug[4][(256 + 32) * 8];
  __shared__ __attribute__((aligned(16))) u16 hp[4][2048 + 16];
  int wave = threadIdx.x >> 6, lane = threadIdx.x & 63;
  int ch = blockIdx.x * 4 + wave;
  for (int i = lane; i < 32 * 8; i += 64) ug[wave][i] = 0;
  for (int i = lane; i < 2048; i += 64) ug[wave][256 + (i & ~7) + 7 - (i & 7)] = kvT[ch * S + i];
  if (lane < 16) hp[wave][lane] = 0;
  for (int i = lane; i < 2048; i += 64) hp[wave][16 + i] = ssmk[ch * S + i];
  __syncthreads();
  int n = lane & 15, qq = lane >> 4;
  f32x4 acc[8];
  for (int c = 0; c < 8; ++c) acc[c] = (f32x4){0, 0, 0, 0};
  for (int m0 = 0; m0 < 8; ++m0) {
    bf16x8 A[8], B[8];
#pragma unroll
    for (int i = 0; i < 8; ++i) {
      int m = m0 + 8 * i;
      int st = n + 1 + 8 * qq + 32 * m;
#pragma unroll
      for (int j = 0; j < 8; ++j) A[i][j] = (short)hp[wave][st + j];
      int delta = 8 * i - m0;
      B[i] = *(const bf16x8*)&ug[wave][(32 + 4 * delta + 2 * n + 1 - qq) * 8];
    }
#pragma unroll
    for (int c = 0; c < 8; ++c)
#pragma unroll
      for (int i = 0; i <= c; ++i) acc[c] = __builtin_amdgcn_mfma_f32_16x16x32_bf16(A[i], B[c - i], acc[c], 0, 0, 0);
  }
  float Dv = bf2f(ssmD[ch]);
  int i0 = selidx[0], i1 = selidx[1];
#pragma unroll
  for (int c = 0; c < 8; ++c)
#pragma unroll
    for (int r = 0; r < 4; ++r) {
      int s = 256 * c + 16 * n + 4 * qq + r;
      float kv = bf2f(ug[wave][256 + (s & ~7) + 7 - (s & 7)]);
      float y = rbf(rbf(acc[c][r]) + rbf(kv * Dv));
      float x = bf2f(q[s * D + ch]);
      float xn = rbf(rbf(x - stats[2 * s]) / stats[2 * s + 1]);
      float oa = rbf(xn * y);
      float ob = xn;
      if (s == i0) ob = rbf(xn + upd[ch]);
      if (s == i1) ob = rbf(xn + upd[D + ch]);
      out[s * D + ch] = f2bf(rbf(x + oa) + ob);
    }
}

extern "C" void kernel_launch(void* const* d_in, const int* in_sizes, int n_in, void* d_out, int out_size, void* d_ws,
                              size_t ws_size, hipStream_t stream) {
  const u16* q = (const u16*)d_in[0];
  const u16* k = (const u16*)d_in[1];
  const u16* v = (const u16*)d_in[2];
  const u16* shk = (const u16*)d_in[3];
  const u16* ssmk = (const u16*)d_in[4];
  const u16* ssmD = (const u16*)d_in[5];
  const u16* wr = (const u16*)d_in[7];
  const u16* wg = (const u16*)d_in[8];
  const u16* wu = (const u16*)d_in[9];
  const u16* wd = (const u16*)d_in[10];
  char* ws = (char*)d_ws;
  float* stats = (float*)ws;
  float* rr = (float*)(ws + 16384);
  float* hbuf = (float*)(ws + 32768);
  int* selidx = (int*)(ws + 65536);
  float* selval = (float*)(ws + 65536 + 64);
  float* upd = (float*)(ws + 65536 + 256);
  u16* kvT = (u16*)(ws + (1 << 20));
  k1<<<256, 256, 0, stream>>>(q, k, v, shk, wr, stats, rr, kvT);
  k2<<<F / 64, 256, 0, stream>>>(q, stats, rr, wg, wu, hbuf, selidx, selval);
  k3<<<D / 64, 256, 0, stream>>>(hbuf, wd, selval, upd);
  k4<<<256, 256, 0, stream>>>(q, kvT, ssmk, ssmD, stats, selidx, upd, (u16*)d_out);
}
```

```cpp
#include <hip/hip_runtime.h>
#include <stdint.h>
#include <utility>

typedef unsigned short u16;
typedef unsigned u32;
typedef unsigned long long u64;
typedef u32 v4u __attribute__((ext_vector_type(4)));
typedef u32 v2u __attribute__((ext_vector_type(2)));
typedef float f32x4 __attribute__((ext_vector_type(4)));
typedef float f32x2 __attribute__((ext_vector_type(2)));
typedef __bf16 bf16x8 __attribute__((ext_vector_type(8)));
typedef __bf16 bf16x2 __attribute__((ext_vector_type(2)));
typedef short s16x4 __attribute__((ext_vector_type(4)));
#define LAS __attribute__((address_space(3)))
#define CAS __attribute__((address_space(4)))
typedef __amdgpu_buffer_rsrc_t rsrc_t;

#define POL_IN 0
#define POL_Q 0
#define POL_W 0
#define WIN 10
#define DLY 3
#define NRES 4
#define EARLY_W 0
#define PD 4
#define CODE_KB 26
#define SC1 16
#define KXAUX 16
#define NO_W 0
#define KXL 1

constexpr int S = 2048, D = 1024, F = 3584;
constexpr u32 SENT = 0xFFFFFFFFu;
constexpr u32 OOB = 0x80000000u;
constexpr u32 SPIN_G = 1u << 16;
constexpr u32 SPIN_L = 1u << 22;

constexpr u32 WS_EPOCH = 0;
constexpr u32 WS_REC = 4096;
constexpr u32 WS_KVF = WS_REC + 32768;
constexpr u32 WS_GU = WS_KVF + 131072;
constexpr u32 WS_I2B = 1u << 20;
constexpr u32 WS_KX = 4u << 20;
constexpr u32 WS_DBG = 12u << 20;
constexpr u32 WS_END = 13u << 20;

constexpr u32 HC_COPY = 4160, HC_CH = 4 * HC_COPY;
constexpr u32 L_HC = 0;
constexpr u32 UB_CH = 512 + 4096 + 64 + 4096 + 64;
constexpr u32 L_UB = L_HC + 4 * HC_CH;
constexpr u32 OS_CH = 4224;
constexpr u32 L_OS = L_UB + 4 * UB_CH;
constexpr u32 L_RS = L_OS + 4 * OS_CH;
constexpr u32 L_NB = L_RS + 8192;
constexpr u32 L_XS = L_NB + 8192;
constexpr u32 L_HA = L_XS + 1024;
constexpr u32 L_LN = L_HA + 512;
constexpr u32 L_CTL = L_LN + 64;
constexpr u32 C_OUT = L_CTL;
constexpr u32 C_SEL = L_CTL + 16;
constexpr u32 C_LN = L_CTL + 32;
constexpr u32 C_PUB = L_CTL + 48;
constexpr u32 C_STG = L_CTL + 64;
constexpr u32 C_STA = L_CTL + 68;
constexpr u32 C_TAG = L_CTL + 72;
constexpr u32 C_ZERO = L_CTL + 80;
constexpr u32 C_DN = L_CTL + 96;
constexpr u32 L_END = L_CTL + 128;

struct Params {
  const u16 *q, *k, *v, *shk, *ssmk, *ssmD, *wr, *wg, *wu, *wd;
  u16* out;
  char* ws;
};

__device__ __forceinline__ rsrc_t mkrs(const void* p, u32 bytes) {
  return __builtin_amdgcn_make_buffer_rsrc((void*)p, 0, bytes, 0x00020000);
}
template <int AUX> __device__ __forceinline__ v4u ld16(rsrc_t r, u32 off) { return __builtin_amdgcn_raw_buffer_load_b128(r, off, 0, AUX); }
template <int AUX> __device__ __forceinline__ v2u ld8(rsrc_t r, u32 off) { return __builtin_amdgcn_raw_buffer_load_b64(r, off, 0, AUX); }
template <int AUX> __device__ __forceinline__ u32 ld4(rsrc_t r, u32 off) { return __builtin_amdgcn_raw_buffer_load_b32(r, off, 0, AUX); }
template <int AUX> __device__ __forceinline__ void st16(v4u v, rsrc_t r, u32 off) { __builtin_amdgcn_raw_buffer_store_b128(v, r, off, 0, AUX); }
template <int AUX> __device__ __forceinline__ void st8(v2u v, rsrc_t r, u32 off) { __builtin_amdgcn_raw_buffer_store_b64(v, r, off, 0, AUX); }
template <int AUX> __device__ __forceinline__ void st4(u32 v, rsrc_t r, u32 off) { __builtin_amdgcn_raw_buffer_store_b32(v, r, off, 0, AUX); }

__device__ __forceinline__ float asf(u32 x) { return __builtin_bit_cast(float, x); }
__device__ __forceinline__ u32 asu(float x) { return __builtin_bit_cast(u32, x); }
__device__ __forceinline__ float lo2f(u32 x) { return asf(x << 16); }
__device__ __forceinline__ float hi2f(u32 x) { return asf(x & 0xFFFF0000u); }
__device__ __forceinline__ u32 pk2(float lo, float hi) {
  f32x2 f = {lo, hi};
  return __builtin_bit_cast(u32, __builtin_convertvector(f, bf16x2));
}
__device__ __forceinline__ float rbf(float f) { return lo2f(pk2(f, f)); }
__device__ __forceinline__ float dot2(u32 a, u32 b, float c) {
  return __builtin_amdgcn_fdot2_f32_bf16(__builtin_bit_cast(bf16x2, a), __builtin_bit_cast(bf16x2, b), c, false);
}
__device__ __forceinline__ f32x4 mfma(v4u a, v4u b, f32x4 c) {
  return __builtin_amdgcn_mfma_f32_16x16x32_bf16(__builtin_bit_cast(bf16x8, a), __builtin_bit_cast(bf16x8, b), c, 0, 0, 0);
}
#define SCHED() __builtin_amdgcn_sched_barrier(0)
#define VMCNT(n) do { SCHED(); asm volatile("s_waitcnt vmcnt(%0)" ::"n"(n) : "memory"); \
    __builtin_amdgcn_s_waitcnt(((n) & 15) | (7 << 4) | (15 << 8) | (((n) >> 4) << 14)); SCHED(); } while (0)
#define LGKM0() asm volatile("s_waitcnt lgkmcnt(0)" ::: "memory")
#define MEMFENCE() asm volatile("" ::: "memory")
#define SPIN(cap) _Pragma("unroll 1") for (u32 spins = 0; spins < (cap); ++spins)

template <int CTRL, int RM> __device__ __forceinline__ float dppf(float x) {
  return asf(__builtin_amdgcn_update_dpp(0u, asu(x), CTRL, RM, 0xf, false));
}
template <int CTRL, int RM> __device__ __forceinline__ u32 dppu(u32 x) { return __builtin_amdgcn_update_dpp(0u, x, CTRL, RM, 0xf, false); }
__device__ __forceinline__ float wave_sum63(float x) {
  x += dppf<0x111, 0xf>(x); x += dppf<0x112, 0xf>(x); x += dppf<0x114, 0xf>(x); x += dppf<0x118, 0xf>(x);
  x += dppf<0x142, 0xa>(x); x += dppf<0x143, 0xc>(x);
  return x;
}
__device__ __forceinline__ u32 umax(u32 a, u32 b) { return a > b ? a : b; }
__device__ __forceinline__ u32 umin(u32 a, u32 b) { return a < b ? a : b; }
__device__ __forceinline__ u32 wave_max(u32 x) {
  x = umax(x, dppu<0x111, 0xf>(x)); x = umax(x, dppu<0x112, 0xf>(x)); x = umax(x, dppu<0x114, 0xf>(x)); x = umax(x, dppu<0x118, 0xf>(x));
  x = umax(x, dppu<0x142, 0xa>(x)); x = umax(x, dppu<0x143, 0xc>(x));
  return __builtin_amdgcn_readlane(x, 63);
}
__device__ __forceinline__ u32 med3u(u32 a, u32 b, u32 c) {
  u32 r; asm("v_med3_u32 %0, %1, %2, %3" : "=v"(r) : "v"(a), "v"(b), "v"(c)); return r;
}
__device__ __forceinline__ bool wave_all(bool p) { return __builtin_amdgcn_ballot_w64(p) == ~0ull; }
__device__ __forceinline__ u32 nosent(float f) { return umin(asu(f), SENT - 1u); }
__device__ __forceinline__ u32 rfl(u32 x) { return __builtin_amdgcn_readfirstlane(x); }

template <typename T> __device__ __forceinline__ T lds_ld(u32 a) { return *(LAS T*)(size_t)a; }
template <typename T> __device__ __forceinline__ T lds_ldv(u32 a) { return *(volatile LAS T*)(size_t)a; }
template <typename T> __device__ __forceinline__ void lds_st(u32 a, T v) { *(LAS T*)(size_t)a = v; }
template <typename T> __device__ __forceinline__ void lds_stv(u32 a, T v) { *(volatile LAS T*)(size_t)a = v; }
__device__ __forceinline__ void lds_wait_nz(u32 a) {
  SPIN(SPIN_L) {
    if (rfl(lds_ldv<u32>(a))) break;
    __builtin_amdgcn_s_sleep(1);
  }
  MEMFENCE();
}
__device__ __forceinline__ void lds_wait_min4(u32 a, u32 gt) {
  SPIN(SPIN_L) {
    const v4u f = lds_ldv<v4u>(a);
    if (rfl(umin(umin(f.x, f.y), umin(f.z, f.w))) > gt) break;
    __builtin_amdgcn_s_sleep(1);
  }
  MEMFENCE();
}

#define STAMP(i)
#define STAMP_DECL
#define STAMP_FLUSH()

__device__ __forceinline__ void perm8(v4u* w) {
  v4u o[8];
#pragma unroll
  for (int c = 0; c < 8; ++c)
#pragma unroll
    for (int i = 0; i < 4; ++i)
      o[c][i] = __builtin_amdgcn_perm(w[2 * i + 1][c >> 1], w[2 * i][c >> 1], (c & 1) ? 0x07060302u : 0x05040100u);
#pragma unroll
  for (int c = 0; c < 8; ++c) w[c] = o[c];
}

__device__ __forceinline__ v4u rdA(u32 aA, int m) {
  v2u lo = lds_ldv<v2u>(aA + 64 * m), hi = lds_ldv<v2u>(aA + 64 * m + 8);
  return (v4u){lo.x, lo.y, hi.x, hi.y};
}

constexpr int NSTEP = 160, RING = PD + 2;
constexpr int st_pp(int s) { return s < 16 ? 0 : s < 48 ? 1 : s < 96 ? 2 : 3; }
constexpr int st_b0(int pp) { return pp == 0 ? 0 : pp == 1 ? 16 : pp == 2 ? 48 : 96; }
constexpr int st_ls(int s) { return s - st_b0(st_pp(s)); }
constexpr bool st_diag(int s) { return st_ls(s) >= 16 * st_pp(s); }
constexpr int st_m0(int s) { return st_diag(s) ? (st_ls(s) - 16 * st_pp(s)) / 2 : st_ls(s) / (2 * st_pp(s)); }
constexpr int st_bb(int s) { return st_diag(s) ? 2 * st_pp(s) + (st_ls(s) & 1) : st_ls(s) % (2 * st_pp(s)); }
constexpr bool st_needP(int s) { return s < NSTEP && st_bb(s) == 0 && 2 * st_pp(s) + 1 >= NRES; }
constexpr bool st_needA(int s) { return s < NSTEP && 2 * st_pp(s) - st_bb(s) >= NRES; }
constexpr bool st_light(int s) { return !st_needA(s + PD) && !st_needP(s + PD + 1); }
constexpr int FIN0 = 4;
constexpr int st_piece(int s) {
  if (st_pp(s) == 0 || st_ls(s) < FIN0 || !st_light(s)) return -1;
  int n = 0;
  for (int t = st_b0(st_pp(s)) + FIN0; t < s; ++t) n += st_light(t) ? 1 : 0;
  return n < 10 ? n : -1;
}

__device__ __forceinline__ void mfma_acc(f32x4& c, v4u a, v4u b) {
  asm volatile("v_mfma_f32_16x16x32_bf16 %0, %1, %2, %0" : "+v"(c) : "v"(a), "v"(b));
}
__device__ __forceinline__ void mfma_one(f32x4& c, v4u a, v4u b) {
  asm volatile("v_mfma_f32_16x16x32_bf16 %0, %1, %2, 1.0" : "=&v"(c) : "v"(a), "v"(b));
}

struct Conv {
  v4u A[8 * NRES], AD, Bq[RING];
  v2u Al[RING], Ah[RING], Pl[2], Ph[2];
  f32x4 acc[2][2], rs[2], nb[2];
  float o[4];
  v2u xx[2];
  u32 aA, aB, aX, aS, osw, flg, pk;

  template <int s> __device__ __forceinline__ void rdB() {
    if constexpr (s < NSTEP) Bq[s % RING] = lds_ldv<v4u>(aB + (8 * st_bb(s) - st_m0(s) + 7) * 64);
  }
  template <int s, int h> __device__ __forceinline__ void rdAq() {
    if constexpr (st_needA(s)) {
      constexpr int m = st_m0(s) + 8 * (2 * st_pp(s) - st_bb(s));
      if constexpr (h == 0) Al[s % RING] = lds_ldv<v2u>(aA + 64 * m); else Ah[s % RING] = lds_ldv<v2u>(aA + 64 * m + 8);
    }
  }
  template <int s, int h> __device__ __forceinline__ void rdP() {
    if constexpr (st_needP(s)) {
      constexpr int m = st_m0(s) + 8 * (2 * st_pp(s) + 1);
      if constexpr (h == 0) Pl[st_m0(s) & 1] = lds_ldv<v2u>(aA + 64 * m); else Ph[st_m0(s) & 1] = lds_ldv<v2u>(aA + 64 * m + 8);
    }
  }
  template <int s> __device__ __forceinline__ v4u ringA() {
    const v2u l = Al[s % RING], h = Ah[s % RING];
    return (v4u){l.x, l.y, h.x, h.y};
  }
  template <int pp> __device__ __forceinline__ void fin_load() {
#pragma unroll
    for (int e = 0; e < 2; ++e) {
      xx[e] = lds_ldv<v2u>(aX + (2 * pp + e) * 512);
      rs[e] = lds_ldv<f32x4>(aS + (2 * pp + e) * 1024);
      nb[e] = lds_ldv<f32x4>(aS + (2 * pp + e) * 1024 + 8192);
    }
  }
  template <int pp, int pc> __device__ __forceinline__ void piece() {
    constexpr int e = pc / 5, r = pc % 5;
    if constexpr (r < 4) {
      const u32 xw = xx[e][r >> 1];
      const float x = (r & 1) ? hi2f(xw) : lo2f(xw);
      o[r] = __builtin_fmaf(__builtin_fmaf(x, rs[e][r], nb[e][r]), acc[pp & 1][e][r], x);
      asm volatile("" : "+v"(o[r]));
    } else {
      lds_stv<v2u>(osw + (2 * pp + e) * 512, (v2u){pk2(o[0], o[1]), pk2(o[2], o[3])});
      lds_stv<u32>(flg, 2 * pp + e + 1);
    }
  }
  template <int s> __device__ __forceinline__ void step() {
    constexpr int pp = st_pp(s), m0 = st_m0(s), bb = st_bb(s), c0 = 2 * pp, c1 = c0 + 1, ls = st_ls(s);
    if constexpr (pp > 0 && ls == 16 * pp - PD - 3) pk = lds_ldv<u32>(C_STG);
    if constexpr (pp > 0 && ls == 16 * pp - PD) {
      if (rfl(pk) <= (u32)pp) {
        SPIN(SPIN_L) { if (rfl(lds_ldv<u32>(C_STG)) > (u32)pp) break; __builtin_amdgcn_s_sleep(1); }
      }
    }
    if constexpr (s == 13) pk = lds_ldv<u32>(C_STA);
    if constexpr (s == 16) { if (rfl(pk) == 0) lds_wait_nz(C_STA); }
    const v4u Bf = Bq[s % RING];
    v4u A1, A0;
    if constexpr (c1 - bb < NRES) A1 = A[m0 + 8 * (c1 - bb)];
    else if constexpr (bb == 0) A1 = (v4u){Pl[m0 & 1].x, Pl[m0 & 1].y, Ph[m0 & 1].x, Ph[m0 & 1].y};
    else A1 = ringA<s - 1>();
    if constexpr (bb <= c0) {
      if constexpr (c0 - bb < NRES) A0 = A[m0 + 8 * (c0 - bb)]; else A0 = ringA<s>();
    }
    if constexpr (st_needA(s)) asm volatile("" :: "v"(Ah[s % RING]));
    if constexpr (ls == 0) mfma_one(acc[pp & 1][1], A1, Bf); else mfma_acc(acc[pp & 1][1], A1, Bf);
    rdB<s + PD>();
    rdAq<s + PD, 0>();
    rdP<s + PD + 1, 0>();
    SCHED();
    if constexpr (bb <= c0) {
      if constexpr (ls == 0) mfma_one(acc[pp & 1][0], A0, Bf); else mfma_acc(acc[pp & 1][0], A0, Bf);
    }
    rdAq<s + PD, 1>();
    rdP<s + PD + 1, 1>();
    if constexpr (st_piece(s) >= 0) piece<pp - 1, st_piece(s)>();
    if constexpr (m0 == 0 && bb == c1) mfma_acc(acc[pp & 1][1], AD, Bf);
    if constexpr (m0 == 0 && bb == c0) mfma_acc(acc[pp & 1][0], AD, Bf);
    if constexpr (pp > 0 && ls == 0) fin_load<pp - 1>();
    if constexpr (s == NSTEP - 4) fin_load<3>();
    SCHED();
  }
  template <int... Is> __device__ __forceinline__ void pre(std::integer_sequence<int, Is...>) { (rdB<Is>(), ...); }
  template <int... Is> __device__ __forceinline__ void run(std::integer_sequence<int, Is...>) { (step<Is>(), ...); }
  template <int... Is> __device__ __forceinline__ void last(std::integer_sequence<int, Is...>) { (piece<3, Is>(), ...); }
  __device__ __forceinline__ void stream() {
    pre(std::make_integer_sequence<int, PD>{});
    run(std::make_integer_sequence<int, NSTEP>{});
    asm volatile("s_nop 7" : "+v"(acc[1][0]), "+v"(acc[1][1]));
    last(std::make_integer_sequence<int, 10>{});
  }
  __device__ __forceinline__ void setup(int w, int lane, u32 Dbits) {
    const int M = lane & 15, q4 = lane >> 4;
    const u32 ub = L_UB + w * UB_CH, hb = L_HC + w * HC_CH;
    aA = hb + ((M + 1) & 3) * HC_COPY + ((M + 1) >> 2) * 8 + q4 * 16;
#pragma unroll
    for (int m = 0; m < 8 * NRES; ++m) A[m] = rdA(aA, m);
    const int jd = 15 - M - 8 * q4;
#pragma unroll
    for (int i = 0; i < 4; ++i) AD[i] = jd == 2 * i ? Dbits : jd == 2 * i + 1 ? (Dbits << 16) : 0u;
    aB = ub + 512 - 7 * 64 + (2 * M + 1 - q4) * 16;
    aX = ub + 512 + 4096 + 64 + (16 * M + 4 * q4) * 2;
    aS = L_RS + (16 * M + 4 * q4) * 4;
    osw = L_OS + w * OS_CH + (16 * M + 4 * q4) * 2;
    flg = C_OUT + w * 4;
    asm volatile("" : "+v"(aB), "+v"(aA), "+v"(aX), "+v"(aS), "+v"(flg), "+v"(osw), "+v"(AD));
#pragma unroll
    for (int m = 0; m < 8 * NRES; ++m) asm volatile("" : "+v"(A[m]));
  }
};

#define SLP_REC0 8
#define SLP_REC 4
#define SLP_KV 2

__device__ __forceinline__ u32 get_tag() {
  u32 tag = 0;
  SPIN(SPIN_L) {
    const v2u f = lds_ldv<v2u>(C_TAG);
    tag = rfl(f.y);
    if (rfl(f.x)) break;
    __builtin_amdgcn_s_sleep(1);
  }
  return tag;
}

struct Lander {
  v4u in[4][8];
  u32 aI, landed;
  template <int s> __device__ __forceinline__ void pending() {
#pragma unroll
    for (int t = 0; t < s; ++t)
      if (landed == (u32)t) {
#pragma unroll
        for (int i = 0; i < 8; ++i) lds_st<v4u>(aI + t * 1024 + i * 128, in[t][i]);
        MEMFENCE();
        lds_stv<u32>(C_STG, t + 1);
        MEMFENCE();
        landed = t + 1;
      }
  }
};

__global__ void __launch_bounds__(512, 2) mega(Params p) {
  extern __shared__ __attribute__((aligned(16))) char smem[];
  u64 pc0;
  asm volatile("s_getpc_b64 %0" : "=s"(pc0));
  const int tid = threadIdx.x;
  const int wave = __builtin_amdgcn_readfirstlane(tid >> 6);
  const int lane = tid & 63;
  const int b = blockIdx.x, xcd = b & 7, rk = b >> 3;
  const int L = xcd * 32 + rk;
  STAMP_DECL;
  STAMP(0);
  if (tid < 8) lds_st<v4u>(L_CTL + tid * 16, (v4u){0, 0, 0, 0});
  const rsrc_t rws = mkrs(p.ws, WS_END);
  const rsrc_t rq = mkrs(p.q, S * D * 2);
  const int jw = wave & 3;
  const int cg = L >> 4;
  LGKM0();
  __builtin_amdgcn_s_barrier();
  SCHED();

  if (wave < 4) {
    v4u R[19];
    const int w = jw, rb = L & 15, ch = 4 * L + jw;
    const int g = lane & 7, rr = lane >> 3;
    const rsrc_t rk_ = mkrs(p.k, S * D * 2), rv = mkrs(p.v, S * D * 2), rh = mkrs(p.ssmk, S * D * 2), rs = mkrs(p.shk, D * 8);
    const u32 toff = (u32)((w * 512 + rb * 32 + rr * 4) * 2048 + cg * 128 + g * 16);
    for (int i = 0; i < w; ++i) __builtin_amdgcn_s_sleep(DLY);
    SCHED();
#pragma unroll
    for (int i = 0; i < 4; ++i) R[3 + i] = ld16<POL_IN>(rk_, toff + i * 2048);
#pragma unroll
    for (int i = 0; i < 4; ++i) R[7 + i] = ld16<POL_IN>(rv, toff + i * 2048);
#pragma unroll
    for (int i = 0; i < 3; ++i) R[i] = ld16<0>(rk_, toff - (3 - i) * 2048);
#pragma unroll
    for (int i = 0; i < 4; ++i) R[11 + i] = ld16<0>(rs, (cg * 64 + g * 8) * 8 + i * 16);
#pragma unroll
    for (int i = 0; i < 4; ++i) R[15 + i] = ld16<POL_Q>(rq, toff + i * 2048);
    const u32 Dw = *(const CAS u32*)(p.ssmD + (ch & ~1));
    SCHED();
    const u32 ub = L_UB + w * UB_CH, hb = L_HC + w * HC_CH;
    if (lane < 32) lds_st<v4u>(ub + lane * 16, (v4u){0, 0, 0, 0});
    STAMP(1);
    VMCNT(4);
    STAMP(2);
    const u32 tw = hb + g * 1040 + (rr >> 1) * 16;
#pragma unroll
    for (int c = 0; c < 8; ++c) {
      const u32 wa = R[11 + (c >> 1)][(c & 1) * 2], wb = R[11 + (c >> 1)][(c & 1) * 2 + 1];
      const u32 sel = (c & 1) ? 0x07060302u : 0x05040100u;
      u32 P[6];
      float kv[4];
#pragma unroll
      for (int i = 0; i < 6; ++i) P[i] = __builtin_amdgcn_perm(R[i][c >> 1], R[i + 1][c >> 1], sel);
#pragma unroll
      for (int i = 0; i < 4; ++i) {
        const u32 vx = R[7 + i][c >> 1];
        kv[i] = dot2(P[i + 2], wa, dot2(P[i], wb, 0.f)) * ((c & 1) ? hi2f(vx) : lo2f(vx));
      }
      lds_st<v2u>(tw + c * 128 + ((rr & 1) ? 0 : 8), (v2u){pk2(kv[3], kv[2]), pk2(kv[1], kv[0])});
    }
    SCHED();
#pragma unroll
    for (int c = 0; c < 8; ++c) {
      const u32 sel = (c & 1) ? 0x07060302u : 0x05040100u;
      lds_st<v2u>(tw + c * 128 + 64 + ((rr & 1) ? 8 : 0),
                  (v2u){__builtin_amdgcn_perm(R[16][c >> 1], R[15][c >> 1], sel), __builtin_amdgcn_perm(R[18][c >> 1], R[17][c >> 1], sel)});
    }
    MEMFENCE();
#pragma unroll
    for (int i = 0; i < 8; ++i) {
      v4u t = lds_ld<v4u>(hb + i * 1040 + lane * 16);
      st16<KXAUX>(t, rws, WS_KX + (cg * 64 + w * 16 + rb) * 8192 + i * 1024 + lane * 16);
    }
    SCHED();
    v4u hh[4];
    v2u hn[4];
#pragma unroll
    for (int i = 0; i < 4; ++i) hh[i] = ld16<POL_IN>(rh, ch * 4096 + (i * 64 + lane) * 16);
#pragma unroll
    for (int i = 0; i < 4; ++i) hn[i] = ld8<0>(rh, ch * 4096 + (i * 64 + lane) * 16 + 16);
    STAMP(3);
    const u32 tag = get_tag();
    VMCNT(8);
    st4<SC1>(tag, rws, lane == 0 ? WS_KVF + (cg * 64 + w * 16 + rb) * 128 : OOB);
    lds_stv<u32>(C_PUB + w * 4, 1u);
    STAMP(4);
    SCHED();
#pragma unroll
    for (int i = 0; i < 4; ++i) {
      const v4u d = hh[i];
      const u32 e0 = __builtin_amdgcn_alignbit(d.y, d.x, 16), e1 = __builtin_amdgcn_alignbit(d.z, d.y, 16),
                e2 = __builtin_amdgcn_alignbit(d.w, d.z, 16), e3 = __builtin_amdgcn_alignbit(hn[i].x, d.w, 16),
                e4 = __builtin_amdgcn_alignbit(hn[i].y, hn[i].x, 16);
      const u32 a = hb + 32 + (i * 64 + lane) * 16;
      lds_st<v4u>(a, d);
      lds_st<v4u>(a + HC_COPY, (v4u){e0, e1, e2, e3});
      lds_st<v4u>(a + 2 * HC_COPY, (v4u){d.y, d.z, d.w, hn[i].x});
      lds_st<v4u>(a + 3 * HC_COPY, (v4u){e1, e2, e3, e4});
    }
    {
      const u32 s0 = rfl(hh[0].x), s1 = rfl(hh[0].y);
      const int cb = lane & 3;
      const u32 w3 = cb == 0 ? 0u : cb == 1 ? (s0 << 16) : cb == 2 ? s0 : ((s0 >> 16) | (s1 << 16));
      const v4u z = {0, 0, lane == 7 ? (s0 << 16) : 0u, (lane & 4) ? w3 : 0u};
      if (lane < 8) lds_st<v4u>(hb + cb * HC_COPY + (lane >> 2) * 16, z);
    }
    MEMFENCE();
    SCHED();
    Conv cv;
    cv.setup(w, lane, (ch & 1) ? (Dw >> 16) : (Dw & 0xFFFFu));
    STAMP(5);
    lds_wait_nz(C_STG);
    STAMP(6);
    cv.stream();
    STAMP(7);
    STAMP_FLUSH();
    return;
  }

  const bool ffn = rk < 28;
  const int j = xcd * 28 + rk;
  v4u R[48];
  u32 codepf = 0, epoch = 0;
  {
    const rsrc_t rr = mkrs(p.wr, D * 2);
    if (jw == 3) {
      epoch = *(const CAS u32*)(p.ws + WS_EPOCH);
      codepf = ld4<0>(mkrs((const void*)(pc0 & ~255ull), CODE_KB * 1024), (rk & 3) * 8192 + lane * 128);
    }
#pragma unroll
    for (int i = 0; i < 4; ++i) R[40 + i] = ld16<POL_Q>(rq, (8 * L + 2 * jw) * 2048 + i * 1024 + lane * 16);
#pragma unroll
    for (int i = 0; i < 2; ++i) R[44 + i] = ld16<0>(rr, i * 1024 + lane * 16);
    SCHED();
    const v4u sent = {SENT, SENT, SENT, SENT};
    u32 c0off = OOB, c1off = OOB;
    if (jw < 2) { if (ffn && lane < 32) c0off = WS_GU + j * 1024 + jw * 512 + lane * 16; }
    else if (jw == 2) {
      c0off = WS_I2B + L * 8192 + (lane >> 1) * 128 + (lane & 1) * 16;
      if (lane < 48) c1off = c0off + 4096;
    }
    st16<SC1>(sent, rws, c0off);
    st16<SC1>(sent, rws, c1off);
    SCHED();
  }
  const rsrc_t rw = mkrs(jw == 0 ? p.wg : p.wu, (ffn && jw < 2 && !NO_W) ? D * F * 2 : 0);
  const u32 wo = ((j & 3) * 256 + (lane >> 3) * 8) * (F * 2) + (j >> 2) * 128 + (lane & 7) * 16;
#pragma unroll
  for (int i = 0; i < EARLY_W; ++i) R[i] = ld16<POL_W>(rw, wo + ((i >> 3) * 64 + (i & 7)) * (F * 2));
  SCHED();
  asm volatile("" : "+s"(epoch));
  if (jw == 3) lds_stv<v2u>(C_TAG, (v2u){1u, epoch + 1u});
  STAMP(1);
  {
    float sa = 0.f, sb = 0.f, qa = 0.f, qb = 0.f, ra = 0.f, rb_ = 0.f, sw = 0.f;
#pragma unroll
    for (int i = 0; i < 8; ++i) {
      const u32 a = R[40 + (i >> 2)][i & 3], bq = R[42 + (i >> 2)][i & 3], wv = R[44 + (i >> 2)][i & 3];
      sa = dot2(a, 0x3f803f80u, sa); sb = dot2(bq, 0x3f803f80u, sb);
      qa = dot2(a, a, qa); qb = dot2(bq, bq, qb);
      ra = dot2(a, wv, ra); rb_ = dot2(bq, wv, rb_);
      sw = dot2(wv, 0x3f803f80u, sw);
    }
    sa = wave_sum63(sa); sb = wave_sum63(sb); qa = wave_sum63(qa); qb = wave_sum63(qb);
    ra = wave_sum63(ra); rb_ = wave_sum63(rb_); sw = wave_sum63(sw);
    const float ma = sa * (1.f / D), mb = sb * (1.f / D);
    const float va = __builtin_fmaxf(__builtin_fmaf(-ma, ma, qa * (1.f / D)), 0.f), vb = __builtin_fmaxf(__builtin_fmaf(-mb, mb, qb * (1.f / D)), 0.f);
    const u32 mm = pk2(ma, mb), v1 = pk2(va, vb);
    const u32 v2 = pk2(lo2f(v1) + 1e-5f, hi2f(v1) + 1e-5f);
    const u32 sd = pk2(__builtin_amdgcn_sqrtf(lo2f(v2)), __builtin_amdgcn_sqrtf(hi2f(v2)));
    u32 rp = pk2(__builtin_fmaf(-lo2f(mm), sw, ra) * __builtin_amdgcn_rcpf(lo2f(sd)),
                 __builtin_fmaf(-hi2f(mm), sw, rb_) * __builtin_amdgcn_rcpf(hi2f(sd)));
    const u32 neg = ((rp >> 15) & 0x00010001u) * 0xFFFFu;
    rp = rp ^ (neg | 0x80008000u);
    const u32 inv = 2047u - (u32)(8 * L + 2 * jw);
    v4u rec = {(mm & 0xFFFFu) | (sd << 16), (mm >> 16) | (sd & 0xFFFF0000u), (rp << 16) | inv, (rp & 0xFFFF0000u) | (inv - 1u)};
    asm volatile("" : "+v"(rec) : "v"(codepf));
    VMCNT(EARLY_W);
    if (lane == 63) {
      lds_st<v4u>(L_LN + jw * 16, rec);
      MEMFENCE();
      lds_stv<u32>(C_LN + jw * 4, 1u);
    }
    MEMFENCE();
  }
  STAMP(2);

  if (jw < 2) {
    if (!ffn) { STAMP_FLUSH(); return; }
    const int q4 = lane >> 4, N = lane & 15;
    const rsrc_t rd = mkrs(p.wd, NO_W ? 0 : D * F * 2);
    const u32 dofs = ((j >> 2) * 64 + q4 * 8) * 2048 + (j & 3) * 512 + jw * 256 + N * 16;
#pragma unroll
    for (int i = EARLY_W; i < 48; ++i) {
      if (i >= WIN && i < 32 + WIN) VMCNT(WIN - 1);
      if (i < 32) R[i] = ld16<POL_W>(rw, wo + ((i >> 3) * 64 + (i & 7)) * (F * 2));
      else R[i] = ld16<POL_W>(rd, dofs + (((i >> 3) & 1) * 32 + (i & 7)) * 2048);
      SCHED();
      if (i >= WIN + 8 && ((i - WIN) & 7) == 0 && i - WIN <= 24) { perm8(&R[i - WIN - 8]); SCHED(); }
    }
    static_assert(WIN <= 16 && WIN >= EARLY_W, "");
    perm8(&R[24]);
#pragma unroll
    for (int i = 0; i < 32; ++i) asm volatile("" : "+v"(R[i]));
    u32 ax = L_XS + ((lane >> 1) & 1) * 512 + q4 * 32 + (lane & 1) * 16;
    const bool kap = (lane >> 3) & 1;
    u32 oo = lane < 16 ? WS_GU + j * 1024 + jw * 512 + (lane & 7) * 64 + (kap ? 32 : 0) : OOB;
    asm volatile("" : "+v"(ax), "+v"(oo));
    STAMP(3);
    lds_wait_nz(C_SEL);
    __builtin_amdgcn_s_setprio(2);
    STAMP(4);
    {
      f32x4 acc[8];
#pragma unroll
      for (int c = 0; c < 8; ++c) acc[c] = (f32x4){0, 0, 0, 0};
#pragma unroll
      for (int bk = 0; bk < 4; ++bk) {
        const v4u X = lds_ld<v4u>(ax + bk * 128);
#pragma unroll
        for (int c = 0; c < 8; ++c) acc[c] = mfma(X, R[bk * 8 + c], acc[c]);
      }
      u32 o[8];
#pragma unroll
      for (int cc = 0; cc < 4; ++cc)
#pragma unroll
        for (int t = 0; t < 2; ++t) {
          const float keep = kap ? acc[4 + cc][2 * t + 1] : acc[cc][2 * t], send = kap ? acc[cc][2 * t + 1] : acc[4 + cc][2 * t];
          o[cc * 2 + t] = nosent(keep + dppf<0x128, 0xf>(send));
        }
      st16<SC1>((v4u){o[0], o[1], o[2], o[3]}, rws, oo);
      st16<SC1>((v4u){o[4], o[5], o[6], o[7]}, rws, oo + 16);
    }
    STAMP(5);
    SCHED();
    perm8(&R[32]);
    perm8(&R[40]);
#pragma unroll
    for (int i = 32; i < 48; ++i) asm volatile("" : "+v"(R[i]));
    const u32 ha = L_HA + jw * 256;
    u32 ah[2][2];
#pragma unroll
    for (int hf = 0; hf < 2; ++hf)
#pragma unroll
      for (int ks = 0; ks < 2; ++ks) {
        ah[hf][ks] = (N >> 1) == hf ? ha + (N & 1) * 128 + ks * 64 + q4 * 16 : C_ZERO;
        asm volatile("" : "+v"(ah[hf][ks]));
      }
    u32 o2 = N < 4 ? WS_I2B + ((j & 3) * 64 + jw * 32 + q4 * 8 + (N >> 1)) * 8192 + (j >> 2) * 128 + (N & 1) * 16 : OOB;
    u32 po = WS_GU + (j & ~3) * 1024 + lane * 8;
    asm volatile("" : "+v"(o2), "+v"(po));
    STAMP(6);
    v2u pv[8];
    SPIN(SPIN_G) {
      MEMFENCE();
      u32 mx = 0;
#pragma unroll
      for (int i = 0; i < 8; ++i) pv[i] = ld8<SC1>(rws, po + i * 512);
#pragma unroll
      for (int i = 0; i < 8; ++i) mx = umax(mx, umax(pv[i].x, pv[i].y));
      if (wave_all(mx != SENT)) break;
      __builtin_amdgcn_s_sleep(2);
    }
    STAMP(7);
    const float g0 = (asf(pv[0].x) + asf(pv[2].x)) + (asf(pv[4].x) + asf(pv[6].x)), g1 = (asf(pv[0].y) + asf(pv[2].y)) + (asf(pv[4].y) + asf(pv[6].y));
    const float u0 = (asf(pv[1].x) + asf(pv[3].x)) + (asf(pv[5].x) + asf(pv[7].x)), u1 = (asf(pv[1].y) + asf(pv[3].y)) + (asf(pv[5].y) + asf(pv[7].y));
    const u32 gp = pk2(g0, g1), up = pk2(u0, u1);
    const float ga = lo2f(gp), gb = hi2f(gp);
    const u32 sp = pk2(ga * __builtin_amdgcn_rcpf(1.f + __expf(-ga)), gb * __builtin_amdgcn_rcpf(1.f + __expf(-gb)));
    const u32 hp = pk2(lo2f(sp) * lo2f(up), hi2f(sp) * hi2f(up));
    lds_st<u16>(ha + lane * 2, (u16)hp);
    lds_st<u16>(ha + 128 + lane * 2, (u16)(hp >> 16));
    MEMFENCE();
    f32x4 acc[4];
#pragma unroll
    for (int c = 0; c < 4; ++c) acc[c] = (f32x4){0, 0, 0, 0};
#pragma unroll
    for (int ks = 0; ks < 2; ++ks)
#pragma unroll
      for (int hf = 0; hf < 2; ++hf) {
        const v4u Hb = lds_ld<v4u>(ah[hf][ks]);
#pragma unroll
        for (int cc = 0; cc < 4; ++cc) acc[cc] = mfma(R[32 + ks * 8 + hf * 4 + cc], Hb, acc[cc]);
      }
#pragma unroll
    for (int r = 0; r < 4; ++r)
      st16<SC1>((v4u){nosent(acc[0][r]), nosent(acc[1][r]), nosent(acc[2][r]), nosent(acc[3][r])}, rws, o2 + r * 2 * 8192);
    lds_stv<u32>(C_DN, 1u);
    STAMP(8);
    STAMP_FLUSH();
    return;
  }
  if (jw == 2) {
    __builtin_amdgcn_s_setprio(2);
    {
      const u32 tag = get_tag();
      Lander ld;
      ld.aI = L_UB + ((lane >> 3) & 3) * UB_CH + 512 + ((lane & 4) ? 4096 : 0) + (lane >> 5) * 64 + (lane & 7) * 16;
      const u32 io = WS_KX + (cg * 64 + (lane >> 5)) * 8192 + (L & 15) * 512 + (lane & 31) * 16;
#define IOFF(s_, i) (((s_) * 16 + (i) * 2) * 8192)
      ld.landed = 0;
      const u32 kfo = WS_KVF + (cg * 64 + lane) * 128, pubo = C_PUB + (lane >> 4) * 4;
      bool seen = false;
#define STAGE(s_) \
      SPIN(SPIN_G * 4) { \
        if (((__builtin_amdgcn_ballot_w64(seen) >> (16 * s_)) & 0xFFFFull) == 0xFFFFull) break; \
        const bool act = (lds_ldv<u32>(pubo) != 0) & !seen; \
        MEMFENCE(); \
        const u32 f = ld4<SC1>(rws, act ? kfo : OOB); \
        ld.pending<s_>(); \
        seen = seen | (act & (f == tag)); \
        __builtin_amdgcn_s_sleep(SLP_KV); \
      } \
      MEMFENCE(); \
      STAMP(3 + s_); \
      _Pragma("unroll") for (int i = 0; i < 8; ++i) ld.in[s_][i] = ld16<SC1>(rws, io + IOFF(s_, i)); \
      SCHED();
      STAGE(0) STAGE(1) STAGE(2) STAGE(3)
#undef STAGE
      ld.pending<4>();
    }
    STAMP(7);
    lds_wait_nz(C_SEL);
    const v4u ks4 = lds_ldv<v4u>(C_SEL);
    const u32 kk = (lane & 1) ? ks4.z : ks4.y;
    const u32 frow = 2047u - (kk & 0x7FFu), kx = kk >> 16;
    const float rsel = lo2f((kx & 0x8000u) ? (kx ^ 0x8000u) : (kx ^ 0xFFFFu));
    float sig = rbf(__builtin_amdgcn_rcpf(1.f + __expf(-rsel)));
    u32 pa = WS_I2B + L * 8192 + (lane >> 1) * 128 + (lane & 1) * 16;
    u32 pb_ = lane < 48 ? pa + 4096 : OOB;
    asm volatile("" : "+v"(pa), "+v"(pb_), "+v"(sig));
    if (ffn) lds_wait_nz(C_DN); else __builtin_amdgcn_s_sleep(40);
    __builtin_amdgcn_s_sleep(4);
    v4u e0, e1;
    SPIN(SPIN_G) {
      MEMFENCE();
      e0 = ld16<SC1>(rws, pa); e1 = ld16<SC1>(rws, pb_);
      const u32 mx = umax(umax(umax(e0.x, e0.y), umax(e0.z, e0.w)), umax(umax(e1.x, e1.y), umax(e1.z, e1.w)));
      if (wave_all(mx != SENT)) break;
      __builtin_amdgcn_s_sleep(2);
    }
    STAMP(8);
    float y[4];
#pragma unroll
    for (int c = 0; c < 4; ++c) {
      const u32 ua = e0[c], ub_ = e1[c];
      float s = asf(ua) + asf(ub_);
      s += dppf<0x122, 0xf>(s); s += dppf<0x124, 0xf>(s); s += dppf<0x128, 0xf>(s);
      const auto r16 = __builtin_amdgcn_permlane16_swap(asu(s), asu(s), false, false);
      s = asf(r16[0]) + asf(r16[1]);
      const auto r32 = __builtin_amdgcn_permlane32_swap(asu(s), asu(s), false, false);
      y[c] = rbf(rbf(asf(r32[0]) + asf(r32[1])) * sig);
    }
    lds_wait_min4(C_OUT, (2047u - (umin(rfl(ks4.y) & 0x7FFu, rfl(ks4.z) & 0x7FFu))) >> 8);
    float o[4];
#pragma unroll
    for (int c = 0; c < 4; ++c) o[c] = lo2f(lds_ld<u16>(L_OS + c * OS_CH + frow * 2)) + y[c];
    st8<0>((v2u){pk2(o[0], o[1]), pk2(o[2], o[3])}, mkrs(p.out, S * D * 2), lane < 2 ? frow * 2048 + L * 8 : OOB);
    STAMP(9);
    STAMP_FLUSH();
    return;
  }

  const u32 tag = epoch + 1u;
  __builtin_amdgcn_s_setprio(2);
  lds_wait_min4(C_LN, 0);
  {
    const v4u r4 = lds_ld<v4u>(L_LN + (lane & 3) * 16);
    u32 a1 = umax(r4.z, r4.w), a2 = umin(r4.z, r4.w);
    u32 b1 = dppu<0xB1, 0xf>(a1), b2 = dppu<0xB1, 0xf>(a2);
    a2 = umax(umin(a1, b1), umax(a2, b2)); a1 = umax(a1, b1);
    b1 = dppu<0x4E, 0xf>(a1); b2 = dppu<0x4E, 0xf>(a2);
    a2 = umax(umin(a1, b1), umax(a2, b2)); a1 = umax(a1, b1);
    const v4u o = lane < 4 ? (v4u){r4.x, tag, r4.y, tag} : (v4u){a1, tag, a2, tag};
    st16<SC1>(o, rws, lane < 4 ? WS_REC + L * 128 + 16 + lane * 16 : lane == 4 ? WS_REC + L * 128 : OOB);
  }
  STAMP(3);
  u32 k1 = 0, k2 = 0;
  {
    u32 go[4];
#pragma unroll
    for (int i = 0; i < 4; ++i) go[i] = WS_REC + (i * 64 + lane) * 128;
    __builtin_amdgcn_s_sleep(SLP_REC0);
    SPIN(SPIN_G) {
      MEMFENCE();
      v4u gr[4];
#pragma unroll
      for (int i = 0; i < 4; ++i) gr[i] = ld16<SC1>(rws, go[i]);
      u32 pend = OOB;
#pragma unroll
      for (int i = 0; i < 4; ++i) {
        const bool ok = (gr[i].y == tag) & (gr[i].w == tag) & (go[i] != OOB);
        const u32 ka = ok ? gr[i].x : 0u, kb = ok ? gr[i].z : 0u;
        k2 = umax(med3u(k1, k2, ka), kb); k1 = umax(k1, ka);
        go[i] = ok ? OOB : go[i];
        pend = umin(pend, go[i]);
      }
      if (wave_all(pend == OOB)) break;
      __builtin_amdgcn_s_sleep(SLP_REC);
    }
  }
  STAMP(4);
  const u32 t1 = wave_max(k1);
  const u32 t2 = wave_max(k1 == t1 ? k2 : k1);
  const u32 idx0 = 2047u - (t1 & 0x7FFu), idx1 = 2047u - (t2 & 0x7FFu);
  const u32 myrow = (lane >> 5) ? idx1 : idx0;
  const u32 so = WS_REC + (myrow >> 3) * 128 + 16 + (myrow & 7) * 8;
  const v4u xs = ld16<0>(rq, myrow * 2048 + (j & 3) * 512 + (lane & 31) * 16);
  v2u sg = ld8<SC1>(rws, so);
  v4u stt[16];
#pragma unroll
  for (int i = 0; i < 16; ++i) stt[i] = ld16<SC1>(rws, WS_REC + (i * 16 + (lane >> 2)) * 128 + 16 + (lane & 3) * 16);
  st4<SC1>(tag, rws, (L == 0 && lane == 0) ? WS_EPOCH : OOB);
  SCHED();
  if (!wave_all(sg.y == tag)) {
    SPIN(SPIN_G) {
      MEMFENCE();
      sg = ld8<SC1>(rws, so);
      if (wave_all(sg.y == tag)) break;
    }
  }
  {
    const float mu = lo2f(sg.x), rs = __builtin_amdgcn_rcpf(hi2f(sg.x));
    v4u xn;
#pragma unroll
    for (int i = 0; i < 4; ++i) { const u32 xx = xs[i]; xn[i] = pk2((lo2f(xx) - mu) * rs, (hi2f(xx) - mu) * rs); }
    lds_st<v4u>(L_XS + lane * 16, xn);
    MEMFENCE();
    lds_stv<v4u>(C_SEL, (v4u){1u, t1, t2, 0u});
    MEMFENCE();
  }
  STAMP(5);
  SPIN(SPIN_G) {
    bool ok = true;
#pragma unroll
    for (int i = 0; i < 16; ++i) ok = ok & (stt[i].y == tag) & (stt[i].w == tag);
    if (wave_all(ok)) break;
    MEMFENCE();
#pragma unroll
    for (int i = 0; i < 16; ++i) stt[i] = ld16<SC1>(rws, WS_REC + (i * 16 + (lane >> 2)) * 128 + 16 + (lane & 3) * 16);
  }
#pragma unroll
  for (int i = 0; i < 16; ++i) {
    const float r0 = __builtin_amdgcn_rcpf(hi2f(stt[i].x)), r1 = __builtin_amdgcn_rcpf(hi2f(stt[i].z));
    lds_st<f32x2>(L_RS + i * 512 + lane * 8, (f32x2){r0, r1});
    lds_st<f32x2>(L_NB + i * 512 + lane * 8, (f32x2){-lo2f(stt[i].x) * r0, -lo2f(stt[i].z) * r1});
  }
  MEMFENCE();
  lds_stv<u32>(C_STA, 1u);
  MEMFENCE();
  STAMP(6);
  {
    const rsrc_t ro = mkrs(p.out, S * D * 2);
    u32 ra = L_OS + ((lane >> 2) & 3) * OS_CH + ((lane >> 4) * 16 + (lane & 3) * 4) * 2;
    u32 row = lane;
#pragma unroll 1
    for (u32 c = 0; c < 8; ++c) {
      lds_wait_min4(C_OUT, c);
      v2u o[4];
#pragma unroll
      for (int i = 0; i < 4; ++i) o[i] = __builtin_bit_cast(v2u, __builtin_amdgcn_ds_read_tr16_b64_v4i16((LAS s16x4*)(size_t)(ra + i * 128)));
#pragma unroll
      for (int i = 0; i < 4; ++i) {
        const u32 r = row + i * 64;
        st8<0>(o[i], ro, ((r == idx0) | (r == idx1)) ? OOB : r * 2048 + L * 8);
      }
      ra += 512; row += 256;
    }
  }
  STAMP(7);
  STAMP_FLUSH();
}

extern "C" void kernel_launch(void* const* d_in, const int* in_sizes, int n_in, void* d_out, int out_size, void* d_ws,
                              size_t ws_size, hipStream_t stream) {
  Params p;
  p.q = (const u16*)d_in[0]; p.k = (const u16*)d_in[1]; p.v = (const u16*)d_in[2];
  p.shk = (const u16*)d_in[3]; p.ssmk = (const u16*)d_in[4]; p.ssmD = (const u16*)d_in[5];
  p.wr = (const u16*)d_in[7]; p.wg = (const u16*)d_in[8]; p.wu = (const u16*)d_in[9]; p.wd = (const u16*)d_in[10];
  p.out = (u16*)d_out; p.ws = (char*)d_ws;
  (void)hipFuncSetAttribute((const void*)mega, hipFuncAttributeMaxDynamicSharedMemorySize, L_END);
  hipLaunchKernelGGL(mega, dim3(256), dim3(512), L_END, stream, p);
}
```

```cpp
#define T_REC 600
#define T_KV0 600
#define T_KVS 0
#include <hip/hip_runtime.h>
#include <stdint.h>
#include <utility>

typedef unsigned short u16;
typedef unsigned u32;
typedef unsigned long long u64;
typedef u32 v4u __attribute__((ext_vector_type(4)));
typedef u32 v2u __attribute__((ext_vector_type(2)));
typedef float f32x4 __attribute__((ext_vector_type(4)));
typedef float f32x2 __attribute__((ext_vector_type(2)));
typedef __bf16 bf16x8 __attribute__((ext_vector_type(8)));
typedef __bf16 bf16x2 __attribute__((ext_vector_type(2)));
typedef short s16x4 __attribute__((ext_vector_type(4)));
#define LAS __attribute__((address_space(3)))
#define CAS __attribute__((address_space(4)))
typedef __amdgpu_buffer_rsrc_t rsrc_t;

#define POL_IN 0
#define POL_Q 0
#define POL_W 0
#define WIN 10
#define DLY 3
#define NRES 4
#define EARLY_W 0
#define PD 4
#define CODE_KB 29
#define SC1 16
#define KXAUX 16
#define NO_W 0
#define KXL 1

constexpr int S = 2048, D = 1024, F = 3584;
constexpr u32 SENT = 0xFFFFFFFFu;
constexpr u32 OOB = 0x80000000u;
constexpr u32 SPIN_G = 1u << 16;
constexpr u32 SPIN_L = 1u << 22;

constexpr u32 WS_EPOCH = 0;
constexpr u32 WS_REC = 4096;
constexpr u32 WS_KVF = WS_REC + 32768;
constexpr u32 WS_GU = WS_KVF + 131072;
constexpr u32 WS_I2B = 1u << 20;
constexpr u32 WS_KX = 4u << 20;
constexpr u32 WS_DBG = 12u << 20;
constexpr u32 WS_END = 13u << 20;

constexpr u32 HC_COPY = 4160, HC_CH = 4 * HC_COPY;
constexpr u32 L_HC = 0;
constexpr u32 UB_CH = 512 + 4096 + 64 + 4096 + 64;
constexpr u32 L_UB = L_HC + 4 * HC_CH;
constexpr u32 OS_CH = 4224;
constexpr u32 L_OS = L_UB + 4 * UB_CH;
constexpr u32 L_RS = L_OS + 4 * OS_CH;
constexpr u32 L_NB = L_RS + 8192;
constexpr u32 L_XS = L_NB + 8192;
constexpr u32 L_HA = L_XS + 1024;
constexpr u32 L_LN = L_HA + 512;
constexpr u32 L_CTL = L_LN + 64;
constexpr u32 C_OUT = L_CTL;
constexpr u32 C_SEL = L_CTL + 16;
constexpr u32 C_LN = L_CTL + 32;
constexpr u32 C_PUB = L_CTL + 48;
constexpr u32 C_STG = L_CTL + 64;
constexpr u32 C_STA = L_CTL + 68;
constexpr u32 C_TAG = L_CTL + 72;
constexpr u32 C_ZERO = L_CTL + 80;
constexpr u32 C_DN = L_CTL + 96;
constexpr u32 C_HC = L_CTL + 112;
constexpr u32 TS_W = 8320;
constexpr u32 L_END = L_CTL + 128;

struct Params {
  const u16 *q, *k, *v, *shk, *ssmk, *ssmD, *wr, *wg, *wu, *wd;
  u16* out;
  char* ws;
};

__device__ __forceinline__ rsrc_t mkrs(const void* p, u32 bytes) {
  return __builtin_amdgcn_make_buffer_rsrc((void*)p, 0, bytes, 0x00020000);
}
template <int AUX> __device__ __forceinline__ v4u ld16(rsrc_t r, u32 off) { return __builtin_amdgcn_raw_buffer_load_b128(r, off, 0, AUX); }
template <int AUX> __device__ __forceinline__ v2u ld8(rsrc_t r, u32 off) { return __builtin_amdgcn_raw_buffer_load_b64(r, off, 0, AUX); }
template <int AUX> __device__ __forceinline__ u32 ld4(rsrc_t r, u32 off) { return __builtin_amdgcn_raw_buffer_load_b32(r, off, 0, AUX); }
template <int AUX> __device__ __forceinline__ void st16(v4u v, rsrc_t r, u32 off) { __builtin_amdgcn_raw_buffer_store_b128(v, r, off, 0, AUX); }
template <int AUX> __device__ __forceinline__ void st8(v2u v, rsrc_t r, u32 off) { __builtin_amdgcn_raw_buffer_store_b64(v, r, off, 0, AUX); }
template <int AUX> __device__ __forceinline__ void st4(u32 v, rsrc_t r, u32 off) { __builtin_amdgcn_raw_buffer_store_b32(v, r, off, 0, AUX); }

__device__ __forceinline__ float asf(u32 x) { return __builtin_bit_cast(float, x); }
__device__ __forceinline__ u32 asu(float x) { return __builtin_bit_cast(u32, x); }
__device__ __forceinline__ float lo2f(u32 x) { return asf(x << 16); }
__device__ __forceinline__ float hi2f(u32 x) { return asf(x & 0xFFFF0000u); }
__device__ __forceinline__ u32 pk2(float lo, float hi) {
  f32x2 f = {lo, hi};
  return __builtin_bit_cast(u32, __builtin_convertvector(f, bf16x2));
}
__device__ __forceinline__ float rbf(float f) { return lo2f(pk2(f, f)); }
__device__ __forceinline__ float dot2(u32 a, u32 b, float c) {
  return __builtin_amdgcn_fdot2_f32_bf16(__builtin_bit_cast(bf16x2, a), __builtin_bit_cast(bf16x2, b), c, false);
}
__device__ __forceinline__ f32x4 mfma(v4u a, v4u b, f32x4 c) {
  return __builtin_amdgcn_mfma_f32_16x16x32_bf16(__builtin_bit_cast(bf16x8, a), __builtin_bit_cast(bf16x8, b), c, 0, 0, 0);
}
#define SCHED() __builtin_amdgcn_sched_barrier(0)
#define VMCNT(n) do { SCHED(); asm volatile("s_waitcnt vmcnt(%0)" ::"n"(n) : "memory"); \
    __builtin_amdgcn_s_waitcnt(((n) & 15) | (7 << 4) | (15 << 8) | (((n) >> 4) << 14)); SCHED(); } while (0)
#define LGKM0() asm volatile("s_waitcnt lgkmcnt(0)" ::: "memory")
#define MEMFENCE() asm volatile("" ::: "memory")
#define SPIN(cap) _Pragma("unroll 1") for (u32 spins = 0; spins < (cap); ++spins)

template <int CTRL, int RM> __device__ __forceinline__ float dppf(float x) {
  return asf(__builtin_amdgcn_update_dpp(0u, asu(x), CTRL, RM, 0xf, false));
}
template <int CTRL, int RM> __device__ __forceinline__ u32 dppu(u32 x) { return __builtin_amdgcn_update_dpp(0u, x, CTRL, RM, 0xf, false); }
__device__ __forceinline__ float wave_sum63(float x) {
  x += dppf<0x111, 0xf>(x); x += dppf<0x112, 0xf>(x); x += dppf<0x114, 0xf>(x); x += dppf<0x118, 0xf>(x);
  x += dppf<0x142, 0xa>(x); x += dppf<0x143, 0xc>(x);
  return x;
}
__device__ __forceinline__ u32 umax(u32 a, u32 b) { return a > b ? a : b; }
__device__ __forceinline__ u32 umin(u32 a, u32 b) { return a < b ? a : b; }
__device__ __forceinline__ u32 wave_max(u32 x) {
  x = umax(x, dppu<0x111, 0xf>(x)); x = umax(x, dppu<0x112, 0xf>(x)); x = umax(x, dppu<0x114, 0xf>(x)); x = umax(x, dppu<0x118, 0xf>(x));
  x = umax(x, dppu<0x142, 0xa>(x)); x = umax(x, dppu<0x143, 0xc>(x));
  return __builtin_amdgcn_readlane(x, 63);
}
__device__ __forceinline__ u32 med3u(u32 a, u32 b, u32 c) {
  u32 r; asm("v_med3_u32 %0, %1, %2, %3" : "=v"(r) : "v"(a), "v"(b), "v"(c)); return r;
}
__device__ __forceinline__ bool wave_all(bool p) { return __builtin_amdgcn_ballot_w64(p) == ~0ull; }
__device__ __forceinline__ u32 nosent(float f) { return umin(asu(f), SENT - 1u); }
__device__ __forceinline__ u32 rfl(u32 x) { return __builtin_amdgcn_readfirstlane(x); }

template <typename T> __device__ __forceinline__ T lds_ld(u32 a) { return *(LAS T*)(size_t)a; }
template <typename T> __device__ __forceinline__ T lds_ldv(u32 a) { return *(volatile LAS T*)(size_t)a; }
template <typename T> __device__ __forceinline__ void lds_st(u32 a, T v) { *(LAS T*)(size_t)a = v; }
template <typename T> __device__ __forceinline__ void lds_stv(u32 a, T v) { *(volatile LAS T*)(size_t)a = v; }
__device__ __forceinline__ void lds_wait_nz(u32 a) {
  SPIN(SPIN_L) {
    if (rfl(lds_ldv<u32>(a))) break;
    __builtin_amdgcn_s_sleep(1);
  }
  MEMFENCE();
}
__device__ __forceinline__ void lds_wait_min4(u32 a, u32 gt) {
  SPIN(SPIN_L) {
    const v4u f = lds_ldv<v4u>(a);
    if (rfl(umin(umin(f.x, f.y), umin(f.z, f.w))) > gt) break;
    __builtin_amdgcn_s_sleep(1);
  }
  MEMFENCE();
}

#define STAMP(i)
#define STAMP_DECL
#define STAMP_FLUSH()

__device__ __forceinline__ void perm8(v4u* w) {
  v4u o[8];
#pragma unroll
  for (int c = 0; c < 8; ++c)
#pragma unroll
    for (int i = 0; i < 4; ++i)
      o[c][i] = __builtin_amdgcn_perm(w[2 * i + 1][c >> 1], w[2 * i][c >> 1], (c & 1) ? 0x07060302u : 0x05040100u);
#pragma unroll
  for (int c = 0; c < 8; ++c) w[c] = o[c];
}

__device__ __forceinline__ v4u rdA(u32 aA, int m) {
  v2u lo = lds_ldv<v2u>(aA + 64 * m), hi = lds_ldv<v2u>(aA + 64 * m + 8);
  return (v4u){lo.x, lo.y, hi.x, hi.y};
}

constexpr int NSTEP = 160, RING = PD + 2;
constexpr int st_pp(int s) { return s < 16 ? 0 : s < 48 ? 1 : s < 96 ? 2 : 3; }
constexpr int st_b0(int pp) { return pp == 0 ? 0 : pp == 1 ? 16 : pp == 2 ? 48 : 96; }
constexpr int st_ls(int s) { return s - st_b0(st_pp(s)); }
constexpr bool st_diag(int s) { return st_ls(s) >= 16 * st_pp(s); }
constexpr int st_m0(int s) { return st_diag(s) ? (st_ls(s) - 16 * st_pp(s)) / 2 : st_ls(s) / (2 * st_pp(s)); }
constexpr int st_bb(int s) { return st_diag(s) ? 2 * st_pp(s) + (st_ls(s) & 1) : st_ls(s) % (2 * st_pp(s)); }
constexpr bool st_needP(int s) { return s < NSTEP && st_bb(s) == 0 && 2 * st_pp(s) + 1 >= NRES; }
constexpr bool st_needA(int s) { return s < NSTEP && 2 * st_pp(s) - st_bb(s) >= NRES; }
constexpr bool st_light(int s) { return !st_needA(s + PD) && !st_needP(s + PD + 1); }
constexpr int FIN0 = 4;
constexpr int st_piece(int s) {
  if (st_pp(s) == 0 || st_ls(s) < FIN0 || !st_light(s)) return -1;
  int n = 0;
  for (int t = st_b0(st_pp(s)) + FIN0; t < s; ++t) n += st_light(t) ? 1 : 0;
  return n < 10 ? n : -1;
}

__device__ __forceinline__ void mfma_acc(f32x4& c, v4u a, v4u b) {
  asm volatile("v_mfma_f32_16x16x32_bf16 %0, %1, %2, %0" : "+v"(c) : "v"(a), "v"(b));
}
__device__ __forceinline__ void mfma_one(f32x4& c, v4u a, v4u b) {
  asm volatile("v_mfma_f32_16x16x32_bf16 %0, %1, %2, 1.0" : "=&v"(c) : "v"(a), "v"(b));
}

struct Conv {
  v4u A[8 * NRES], AD, Bq[RING];
  v2u Al[RING], Ah[RING], Pl[2], Ph[2];
  f32x4 acc[2][2], rs[2], nb[2];
  float o[4];
  v2u xx[2];
  u32 aA, aB, aX, aS, osw, flg, pk;

  template <int s> __device__ __forceinline__ void rdB() {
    if constexpr (s < NSTEP) Bq[s % RING] = lds_ldv<v4u>(aB + (8 * st_bb(s) - st_m0(s) + 7) * 64);
  }
  template <int s, int h> __device__ __forceinline__ void rdAq() {
    if constexpr (st_needA(s)) {
      constexpr int m = st_m0(s) + 8 * (2 * st_pp(s) - st_bb(s));
      if constexpr (h == 0) Al[s % RING] = lds_ldv<v2u>(aA + 64 * m); else Ah[s % RING] = lds_ldv<v2u>(aA + 64 * m + 8);
    }
  }
  template <int s, int h> __device__ __forceinline__ void rdP() {
    if constexpr (st_needP(s)) {
      constexpr int m = st_m0(s) + 8 * (2 * st_pp(s) + 1);
      if constexpr (h == 0) Pl[st_m0(s) & 1] = lds_ldv<v2u>(aA + 64 * m); else Ph[st_m0(s) & 1] = lds_ldv<v2u>(aA + 64 * m + 8);
    }
  }
  template <int s> __device__ __forceinline__ v4u ringA() {
    const v2u l = Al[s % RING], h = Ah[s % RING];
    return (v4u){l.x, l.y, h.x, h.y};
  }
  template <int pp> __device__ __forceinline__ void fin_load() {
#pragma unroll
    for (int e = 0; e < 2; ++e) {
      xx[e] = lds_ldv<v2u>(aX + (2 * pp + e) * 512);
      rs[e] = lds_ldv<f32x4>(aS + (2 * pp + e) * 1024);
      nb[e] = lds_ldv<f32x4>(aS + (2 * pp + e) * 1024 + 8192);
    }
  }
  template <int pp, int pc> __device__ __forceinline__ void piece() {
    constexpr int e = pc / 5, r = pc % 5;
    if constexpr (r < 4) {
      const u32 xw = xx[e][r >> 1];
      const float x = (r & 1) ? hi2f(xw) : lo2f(xw);
      o[r] = __builtin_fmaf(__builtin_fmaf(x, rs[e][r], nb[e][r]), acc[pp & 1][e][r], x);
      asm volatile("" : "+v"(o[r]));
    } else {
      lds_stv<v2u>(osw + (2 * pp + e) * 512, (v2u){pk2(o[0], o[1]), pk2(o[2], o[3])});
      lds_stv<u32>(flg, 2 * pp + e + 1);
    }
  }
  template <int s> __device__ __forceinline__ void step() {
    constexpr int pp = st_pp(s), m0 = st_m0(s), bb = st_bb(s), c0 = 2 * pp, c1 = c0 + 1, ls = st_ls(s);
    if constexpr (pp > 0 && ls == 16 * pp - PD - 3) pk = lds_ldv<u32>(C_STG);
    if constexpr (pp > 0 && ls == 16 * pp - PD) {
      if (rfl(pk) <= (u32)pp) {
        SPIN(SPIN_L) { if (rfl(lds_ldv<u32>(C_STG)) > (u32)pp) break; __builtin_amdgcn_s_sleep(1); }
      }
    }
    if constexpr (s == 13) pk = lds_ldv<u32>(C_STA);
    if constexpr (s == 16) { if (rfl(pk) == 0) lds_wait_nz(C_STA); }
    const v4u Bf = Bq[s % RING];
    v4u A1, A0;
    if constexpr (c1 - bb < NRES) A1 = A[m0 + 8 * (c1 - bb)];
    else if constexpr (bb == 0) A1 = (v4u){Pl[m0 & 1].x, Pl[m0 & 1].y, Ph[m0 & 1].x, Ph[m0 & 1].y};
    else A1 = ringA<s - 1>();
    if constexpr (bb <= c0) {
      if constexpr (c0 - bb < NRES) A0 = A[m0 + 8 * (c0 - bb)]; else A0 = ringA<s>();
    }
    if constexpr (st_needA(s)) asm volatile("" :: "v"(Ah[s % RING]));
    if constexpr (ls == 0) mfma_one(acc[pp & 1][1], A1, Bf); else mfma_acc(acc[pp & 1][1], A1, Bf);
    rdB<s + PD>();
    rdAq<s + PD, 0>();
    rdP<s + PD + 1, 0>();
    SCHED();
    if constexpr (bb <= c0) {
      if constexpr (ls == 0) mfma_one(acc[pp & 1][0], A0, Bf); else mfma_acc(acc[pp & 1][0], A0, Bf);
    }
    rdAq<s + PD, 1>();
    rdP<s + PD + 1, 1>();
    if constexpr (st_piece(s) >= 0) piece<pp - 1, st_piece(s)>();
    if constexpr (m0 == 0 && bb == c1) mfma_acc(acc[pp & 1][1], AD, Bf);
    if constexpr (m0 == 0 && bb == c0) mfma_acc(acc[pp & 1][0], AD, Bf);
    if constexpr (pp > 0 && ls == 0) fin_load<pp - 1>();
    if constexpr (s == NSTEP - 4) fin_load<3>();
    SCHED();
  }
  template <int... Is> __device__ __forceinline__ void pre(std::integer_sequence<int, Is...>) { (rdB<Is>(), ...); }
  template <int... Is> __device__ __forceinline__ void run(std::integer_sequence<int, Is...>) { (step<Is>(), ...); }
  template <int... Is> __device__ __forceinline__ void last(std::integer_sequence<int, Is...>) { (piece<3, Is>(), ...); }
  __device__ __forceinline__ void stream() {
    pre(std::make_integer_sequence<int, PD>{});
    run(std::make_integer_sequence<int, NSTEP>{});
    asm volatile("s_nop 7" : "+v"(acc[1][0]), "+v"(acc[1][1]));
    last(std::make_integer_sequence<int, 10>{});
  }
  __device__ __forceinline__ void setup(int w, int lane, u32 Dbits) {
    const int M = lane & 15, q4 = lane >> 4;
    const u32 ub = L_UB + w * UB_CH, hb = L_HC + w * HC_CH;
    aA = hb + ((M + 1) & 3) * HC_COPY + ((M + 1) >> 2) * 8 + q4 * 16;
#pragma unroll
    for (int m = 0; m < 8 * NRES; ++m) A[m] = rdA(aA, m);
    const int jd = 15 - M - 8 * q4;
#pragma unroll
    for (int i = 0; i < 4; ++i) AD[i] = jd == 2 * i ? Dbits : jd == 2 * i + 1 ? (Dbits << 16) : 0u;
    aB = ub + 512 - 7 * 64 + (2 * M + 1 - q4) * 16;
    aX = ub + 512 + 4096 + 64 + (16 * M + 4 * q4) * 2;
    aS = L_RS + (16 * M + 4 * q4) * 4;
    osw = L_OS + w * OS_CH + (16 * M + 4 * q4) * 2;
    flg = C_OUT + w * 4;
    asm volatile("" : "+v"(aB), "+v"(aA), "+v"(aX), "+v"(aS), "+v"(flg), "+v"(osw), "+v"(AD));
#pragma unroll
    for (int m = 0; m < 8 * NRES; ++m) asm volatile("" : "+v"(A[m]));
  }
};

#define T_W 0
#define SLP_ST 2
#define SLP_REC 4
#define SLP_KV 2

__device__ __forceinline__ u32 get_tag() {
  u32 tag = 0;
  SPIN(SPIN_L) {
    const v2u f = lds_ldv<v2u>(C_TAG);
    tag = rfl(f.y);
    if (rfl(f.x)) break;
    __builtin_amdgcn_s_sleep(1);
  }
  return tag;
}

struct Lander {
  v4u in[4][8];
  u32 aI, landed;
  template <int s> __device__ __forceinline__ void pending() {
#pragma unroll
    for (int t = 0; t < s; ++t)
      if (landed == (u32)t) {
#pragma unroll
        for (int i = 0; i < 8; ++i) lds_st<v4u>(aI + t * 1024 + i * 128, in[t][i]);
        MEMFENCE();
        lds_stv<u32>(C_STG, t + 1);
        MEMFENCE();
        landed = t + 1;
      }
  }
};

__device__ __forceinline__ u32 now() { return (u32)__builtin_amdgcn_s_memrealtime(); }
__device__ __forceinline__ void hold(u32 t0, u32 dt) {
  SPIN(4096) { if (now() - t0 >= dt) break; __builtin_amdgcn_s_sleep(1); }
}
template <int AUX> __device__ __forceinline__ v4u ld16s(rsrc_t r, u32 off, u32 soff) { return __builtin_amdgcn_raw_buffer_load_b128(r, off, soff, AUX); }

typedef u32 v16u __attribute__((ext_vector_type(16)));
typedef u32 v8u __attribute__((ext_vector_type(8)));
#define ENDW() do { STAMP_FLUSH(); asm volatile("s_endpgm" : "=v"(T[0]), "=v"(T[1]), "=v"(T[2]), "=v"(T[3]), "=v"(T[4]), "=v"(T[5]), \
    "=v"(T[6]), "=v"(T[7]), "=v"(T[8]), "=v"(T[9]), "=v"(T[10]), "=v"(T[11]), "=v"(hn[0]), "=v"(hn[1]), "=v"(hn[2]), "=v"(hn[3]) :: "memory"); } while (0)

__global__ void __launch_bounds__(512, 2) mega(Params p_unused) {
  extern __shared__ __attribute__((aligned(16))) char smem[];
  u64 pc0;
  v16u ka;
  v8u kb;
  asm volatile("s_load_dwordx16 %1, %3, 0x0\n\ts_load_dwordx8 %2, %3, 0x40\n\ts_getpc_b64 %0"
               : "=&s"(pc0), "=&s"(ka), "=&s"(kb) : "s"(__builtin_amdgcn_kernarg_segment_ptr()));
  const int tid = threadIdx.x;
  const int wave = __builtin_amdgcn_readfirstlane(tid >> 6);
  const int lane = tid & 63;
  const int b = blockIdx.x, xcd = b & 7, rk = b >> 3;
  const u32 codepf = ld4<0>(mkrs((const void*)(pc0 & ~255ull), CODE_KB * 1024), lane < 8 ? (rk & 3) * 8192 + wave * 1024 + lane * 128 : OOB);
  SCHED();
  const u32 t0 = now();
  const int L = xcd * 32 + rk;
  STAMP_DECL;
  STAMP(0);
  lds_st<v4u>(L_CTL + (tid & 7) * 16, (v4u){0, 0, 0, 0});
  const int jw = wave & 3, cg = L >> 4, rb = L & 15, ch = 4 * L + jw;
  const bool isconv = wave < 4;
  const u32 toff = isconv ? (u32)((jw * 512 + rb * 32 + (lane >> 3) * 4) * 2048 + cg * 128 + (lane & 7) * 16) : (u32)(lane * 16);
  const bool ffn = rk < 28;
  const int j = xcd * 28 + rk;
  u32 c0off = ((wave >> 1) == 2 && ffn) ? WS_GU + j * 1024 + jw * 512 + lane * 16 + (lane >> 5) * OOB
              : wave == 6 ? WS_I2B + L * 8192 + (lane >> 1) * 128 + (lane & 1) * 16 : OOB;
  u32 c1off = (wave == 6 && lane < 48) ? c0off + 4096 : OOB;
  asm volatile("" : "+v"(c0off), "+v"(c1off));
  asm volatile("s_waitcnt lgkmcnt(0)" : "+s"(ka), "+s"(kb) :: "memory");
  __builtin_amdgcn_s_barrier();
  SCHED();
  Params p;
#define KP(v, i) (((u64)v[2 * (i) + 1] << 32) | v[2 * (i)])
  p.q = (const u16*)KP(ka, 0); p.k = (const u16*)KP(ka, 1); p.v = (const u16*)KP(ka, 2); p.shk = (const u16*)KP(ka, 3);
  p.ssmk = (const u16*)KP(ka, 4); p.ssmD = (const u16*)KP(ka, 5); p.wr = (const u16*)KP(ka, 6); p.wg = (const u16*)KP(ka, 7);
  p.wu = (const u16*)KP(kb, 0); p.wd = (const u16*)KP(kb, 1); p.out = (u16*)KP(kb, 2); p.ws = (char*)KP(kb, 3);
#undef KP
  const rsrc_t rws = mkrs(p.ws, WS_END);
  v2u hn[4];
  v4u T[12];
  {
    const rsrc_t r1 = mkrs(isconv ? p.k : p.q + (8 * L + 2 * jw) * D, S * D * 2);
    const rsrc_t r2 = mkrs(isconv ? p.v : p.wr, isconv ? S * D * 2 : D * 2);
    const rsrc_t r3 = mkrs(isconv ? p.q : p.ssmk + ch * S, S * D * 2);
    const u32 str = isconv ? 2048 : 1024;
    for (int i = 0; i < (isconv ? jw : 0); ++i) __builtin_amdgcn_s_sleep(DLY);
    SCHED();
#pragma unroll
    for (int i = 0; i < 4; ++i) T[i] = ld16s<POL_IN>(r1, toff, i * str);
#pragma unroll
    for (int i = 0; i < 4; ++i) T[4 + i] = ld16s<POL_IN>(r2, toff, i * str);
    SCHED();
    st16<SC1>((v4u){SENT, SENT, SENT, SENT}, rws, c0off);
    st16<SC1>((v4u){SENT, SENT, SENT, SENT}, rws, c1off);
    SCHED();
#pragma unroll
    for (int i = 0; i < 4; ++i) T[8 + i] = ld16s<POL_IN>(r3, toff, i * str);
    SCHED();
  }

  if (isconv) {
    v4u R[19];
    const int w = jw;
    const int g = lane & 7, rr = lane >> 3;
    const rsrc_t rk_ = mkrs(p.k, S * D * 2), rs = mkrs(p.shk, D * 8);
#pragma unroll
    for (int i = 0; i < 3; ++i) R[i] = ld16<0>(rk_, toff - (3 - i) * 2048);
#pragma unroll
    for (int i = 0; i < 4; ++i) R[11 + i] = ld16<0>(rs, (cg * 64 + g * 8) * 8 + i * 16);
    const u32 Dw = *(const CAS u32*)(p.ssmD + (ch & ~1));
    SCHED();
#pragma unroll
    for (int i = 0; i < 4; ++i) { R[3 + i] = T[i]; R[7 + i] = T[4 + i]; R[15 + i] = T[8 + i]; }
    const u32 hb = L_OS + w * TS_W;
    if (lane < 32) lds_st<v4u>(L_UB + w * UB_CH + lane * 16, (v4u){0, 0, 0, 0});
    STAMP(1);
    VMCNT(0);
    asm volatile("" :: "v"(codepf));
    STAMP(2);
    const u32 tw = hb + g * 1040 + (rr >> 1) * 16;
#pragma unroll
    for (int c = 0; c < 8; ++c) {
      const u32 wa = R[11 + (c >> 1)][(c & 1) * 2], wb = R[11 + (c >> 1)][(c & 1) * 2 + 1];
      const u32 sel = (c & 1) ? 0x07060302u : 0x05040100u;
      u32 P[6];
      float kv[4];
#pragma unroll
      for (int i = 0; i < 6; ++i) P[i] = __builtin_amdgcn_perm(R[i][c >> 1], R[i + 1][c >> 1], sel);
#pragma unroll
      for (int i = 0; i < 4; ++i) {
        const u32 vx = R[7 + i][c >> 1];
        kv[i] = dot2(P[i + 2], wa, dot2(P[i], wb, 0.f)) * ((c & 1) ? hi2f(vx) : lo2f(vx));
      }
      lds_st<v2u>(tw + c * 128 + ((rr & 1) ? 0 : 8), (v2u){pk2(kv[3], kv[2]), pk2(kv[1], kv[0])});
    }
    SCHED();
#pragma unroll
    for (int c = 0; c < 8; ++c) {
      const u32 sel = (c & 1) ? 0x07060302u : 0x05040100u;
      lds_st<v2u>(tw + c * 128 + 64 + ((rr & 1) ? 8 : 0),
                  (v2u){__builtin_amdgcn_perm(R[16][c >> 1], R[15][c >> 1], sel), __builtin_amdgcn_perm(R[18][c >> 1], R[17][c >> 1], sel)});
    }
    MEMFENCE();
#pragma unroll
    for (int i = 0; i < 8; ++i) {
      v4u t = lds_ld<v4u>(hb + i * 1040 + lane * 16);
      st16<KXAUX>(t, rws, WS_KX + (cg * 64 + w * 16 + rb) * 8192 + i * 1024 + lane * 16);
    }
    STAMP(3);
    const u32 tag = get_tag();
    VMCNT(0);
    st4<SC1>(tag, rws, lane == 0 ? WS_KVF + (cg * 64 + w * 16 + rb) * 128 : OOB);
    lds_stv<u32>(C_PUB + w * 4, 1u);
    STAMP(4);
    SCHED();
    lds_wait_nz(C_HC + w * 4);
    Conv cv;
    cv.setup(w, lane, (ch & 1) ? (Dw >> 16) : (Dw & 0xFFFFu));
    STAMP(5);
    lds_wait_nz(C_STG);
    STAMP(6);
    cv.stream();
    STAMP(7);
    ENDW();
  }

  const rsrc_t rq = mkrs(p.q, S * D * 2);
  v4u R[48];
  u32 epoch = 0;
  {
    if (jw == 3) epoch = *(const CAS u32*)(p.ws + WS_EPOCH);
    const rsrc_t rh = mkrs(p.ssmk, S * D * 2);
#pragma unroll
    for (int i = 0; i < 4; ++i) hn[i] = ld8<0>(rh, ch * 4096 + (i * 64 + lane) * 16 + 16);
    SCHED();
  }
  const rsrc_t rw = mkrs(jw == 0 ? p.wg : p.wu, (ffn && jw < 2 && !NO_W) ? D * F * 2 : 0);
  const u32 wo = ((j & 3) * 256 + (lane >> 3) * 8) * (F * 2) + (j >> 2) * 128 + (lane & 7) * 16;
#pragma unroll
  for (int i = 0; i < EARLY_W; ++i) R[i] = ld16<POL_W>(rw, wo + ((i >> 3) * 64 + (i & 7)) * (F * 2));
  SCHED();
  asm volatile("" : "+s"(epoch));
  if (jw == 3) lds_stv<v2u>(C_TAG, (v2u){1u, epoch + 1u});
  STAMP(1);
  {
    float sa = 0.f, sb = 0.f, qa = 0.f, qb = 0.f, ra = 0.f, rb_ = 0.f, sw = 0.f;
#pragma unroll
    for (int i = 0; i < 8; ++i) {
      const u32 a = T[i >> 2][i & 3], bq = T[2 + (i >> 2)][i & 3], wv = T[4 + (i >> 2)][i & 3];
      sa = dot2(a, 0x3f803f80u, sa); sb = dot2(bq, 0x3f803f80u, sb);
      qa = dot2(a, a, qa); qb = dot2(bq, bq, qb);
      ra = dot2(a, wv, ra); rb_ = dot2(bq, wv, rb_);
      sw = dot2(wv, 0x3f803f80u, sw);
    }
    sa = wave_sum63(sa); sb = wave_sum63(sb); qa = wave_sum63(qa); qb = wave_sum63(qb);
    ra = wave_sum63(ra); rb_ = wave_sum63(rb_); sw = wave_sum63(sw);
    const float ma = sa * (1.f / D), mb = sb * (1.f / D);
    const float va = __builtin_fmaxf(__builtin_fmaf(-ma, ma, qa * (1.f / D)), 0.f), vb = __builtin_fmaxf(__builtin_fmaf(-mb, mb, qb * (1.f / D)), 0.f);
    const u32 mm = pk2(ma, mb), v1 = pk2(va, vb);
    const u32 v2 = pk2(lo2f(v1) + 1e-5f, hi2f(v1) + 1e-5f);
    const u32 sd = pk2(__builtin_amdgcn_sqrtf(lo2f(v2)), __builtin_amdgcn_sqrtf(hi2f(v2)));
    u32 rp = pk2(__builtin_fmaf(-lo2f(mm), sw, ra) * __builtin_amdgcn_rcpf(lo2f(sd)),
                 __builtin_fmaf(-hi2f(mm), sw, rb_) * __builtin_amdgcn_rcpf(hi2f(sd)));
    const u32 neg = ((rp >> 15) & 0x00010001u) * 0xFFFFu;
    rp = rp ^ (neg | 0x80008000u);
    const u32 inv = 2047u - (u32)(8 * L + 2 * jw);
    v4u rec = {(mm & 0xFFFFu) | (sd << 16), (mm >> 16) | (sd & 0xFFFF0000u), (rp << 16) | inv, (rp & 0xFFFF0000u) | (inv - 1u)};
    asm volatile("" : "+v"(rec) : "v"(codepf));
    VMCNT(8 + EARLY_W);
    if (lane == 63) {
      lds_st<v4u>(L_LN + jw * 16, rec);
      MEMFENCE();
      lds_stv<u32>(C_LN + jw * 4, 1u);
    }
    MEMFENCE();
  }
  STAMP(2);
  auto build_copies = [&]() {
    const u32 hb = L_HC + jw * HC_CH;
#pragma unroll
    for (int i = 0; i < 4; ++i) {
      const v4u d = T[8 + i];
      const u32 e0 = __builtin_amdgcn_alignbit(d.y, d.x, 16), e1 = __builtin_amdgcn_alignbit(d.z, d.y, 16),
                e2 = __builtin_amdgcn_alignbit(d.w, d.z, 16), e3 = __builtin_amdgcn_alignbit(hn[i].x, d.w, 16),
                e4 = __builtin_amdgcn_alignbit(hn[i].y, hn[i].x, 16);
      const u32 a = hb + 32 + (i * 64 + lane) * 16;
      lds_st<v4u>(a, d);
      lds_st<v4u>(a + HC_COPY, (v4u){e0, e1, e2, e3});
      lds_st<v4u>(a + 2 * HC_COPY, (v4u){d.y, d.z, d.w, hn[i].x});
      lds_st<v4u>(a + 3 * HC_COPY, (v4u){e1, e2, e3, e4});
    }
    {
      const u32 s0 = rfl(T[8].x), s1 = rfl(T[8].y);
      const int cb = lane & 3;
      const u32 w3 = cb == 0 ? 0u : cb == 1 ? (s0 << 16) : cb == 2 ? s0 : ((s0 >> 16) | (s1 << 16));
      const v4u z = {0, 0, lane == 7 ? (s0 << 16) : 0u, (lane & 4) ? w3 : 0u};
      if (lane < 8) lds_st<v4u>(hb + cb * HC_COPY + (lane >> 2) * 16, z);
    }
    MEMFENCE();
    SCHED();
    lds_stv<u32>(C_HC + jw * 4, 1u);
    MEMFENCE();
    SCHED();
  };
  if (jw < 3) build_copies();

  if (jw < 2) {
    if (!ffn) ENDW();
    const int q4 = lane >> 4, N = lane & 15;
    const rsrc_t rd = mkrs(p.wd, NO_W ? 0 : D * F * 2);
    const u32 dofs = ((j >> 2) * 64 + q4 * 8) * 2048 + (j & 3) * 512 + jw * 256 + N * 16;
    if (T_W) hold(t0, T_W);
#pragma unroll
    for (int i = EARLY_W; i < 48; ++i) {
      if (i >= WIN && i < 32 + WIN) VMCNT(WIN - 1);
      if (i < 32) R[i] = ld16<POL_W>(rw, wo + ((i >> 3) * 64 + (i & 7)) * (F * 2));
      else R[i] = ld16<POL_W>(rd, dofs + (((i >> 3) & 1) * 32 + (i & 7)) * 2048);
      SCHED();
      if (i >= WIN + 8 && ((i - WIN) & 7) == 0 && i - WIN <= 24) { perm8(&R[i - WIN - 8]); SCHED(); }
    }
    static_assert(WIN <= 16 && WIN >= EARLY_W, "");
    perm8(&R[24]);
#pragma unroll
    for (int i = 0; i < 32; ++i) asm volatile("" : "+v"(R[i]));
    u32 ax = L_XS + ((lane >> 1) & 1) * 512 + q4 * 32 + (lane & 1) * 16;
    const bool kap = (lane >> 3) & 1;
    u32 oo = lane < 16 ? WS_GU + j * 1024 + jw * 512 + (lane & 7) * 64 + (kap ? 32 : 0) : OOB;
    asm volatile("" : "+v"(ax), "+v"(oo));
    STAMP(3);
    lds_wait_nz(C_SEL);
    __builtin_amdgcn_s_setprio(2);
    STAMP(4);
    {
      f32x4 acc[8];
#pragma unroll
      for (int c = 0; c < 8; ++c) acc[c] = (f32x4){0, 0, 0, 0};
#pragma unroll
      for (int bk = 0; bk < 4; ++bk) {
        const v4u X = lds_ld<v4u>(ax + bk * 128);
#pragma unroll
        for (int c = 0; c < 8; ++c) acc[c] = mfma(X, R[bk * 8 + c], acc[c]);
      }
      u32 o[8];
#pragma unroll
      for (int cc = 0; cc < 4; ++cc)
#pragma unroll
        for (int t = 0; t < 2; ++t) {
          const float keep = kap ? acc[4 + cc][2 * t + 1] : acc[cc][2 * t], send = kap ? acc[cc][2 * t + 1] : acc[4 + cc][2 * t];
          o[cc * 2 + t] = nosent(keep + dppf<0x128, 0xf>(send));
        }
      st16<SC1>((v4u){o[0], o[1], o[2], o[3]}, rws, oo);
      st16<SC1>((v4u){o[4], o[5], o[6], o[7]}, rws, oo + 16);
    }
    STAMP(5);
    SCHED();
    perm8(&R[32]);
    perm8(&R[40]);
#pragma unroll
    for (int i = 32; i < 48; ++i) asm volatile("" : "+v"(R[i]));
    const u32 ha = L_HA + jw * 256;
    u32 ah[2][2];
#pragma unroll
    for (int hf = 0; hf < 2; ++hf)
#pragma unroll
      for (int ks = 0; ks < 2; ++ks) {
        ah[hf][ks] = (N >> 1) == hf ? ha + (N & 1) * 128 + ks * 64 + q4 * 16 : C_ZERO;
        asm volatile("" : "+v"(ah[hf][ks]));
      }
    u32 o2 = N < 4 ? WS_I2B + ((j & 3) * 64 + jw * 32 + q4 * 8 + (N >> 1)) * 8192 + (j >> 2) * 128 + (N & 1) * 16 : OOB;
    u32 po = WS_GU + (j & ~3) * 1024 + lane * 8;
    asm volatile("" : "+v"(o2), "+v"(po));
    STAMP(6);
    v2u pv[8];
    SPIN(SPIN_G) {
      MEMFENCE();
      u32 mx = 0;
#pragma unroll
      for (int i = 0; i < 8; ++i) pv[i] = ld8<SC1>(rws, po + i * 512);
#pragma unroll
      for (int i = 0; i < 8; ++i) mx = umax(mx, umax(pv[i].x, pv[i].y));
      if (wave_all(mx != SENT)) break;
      __builtin_amdgcn_s_sleep(2);
    }
    STAMP(7);
    const float g0 = (asf(pv[0].x) + asf(pv[2].x)) + (asf(pv[4].x) + asf(pv[6].x)), g1 = (asf(pv[0].y) + asf(pv[2].y)) + (asf(pv[4].y) + asf(pv[6].y));
    const float u0 = (asf(pv[1].x) + asf(pv[3].x)) + (asf(pv[5].x) + asf(pv[7].x)), u1 = (asf(pv[1].y) + asf(pv[3].y)) + (asf(pv[5].y) + asf(pv[7].y));
    const u32 gp = pk2(g0, g1), up = pk2(u0, u1);
    const float ga = lo2f(gp), gb = hi2f(gp);
    const u32 sp = pk2(ga * __builtin_amdgcn_rcpf(1.f + __expf(-ga)), gb * __builtin_amdgcn_rcpf(1.f + __expf(-gb)));
    const u32 hp = pk2(lo2f(sp) * lo2f(up), hi2f(sp) * hi2f(up));
    lds_st<u16>(ha + lane * 2, (u16)hp);
    lds_st<u16>(ha + 128 + lane * 2, (u16)(hp >> 16));
    MEMFENCE();
    f32x4 acc[4];
#pragma unroll
    for (int c = 0; c < 4; ++c) acc[c] = (f32x4){0, 0, 0, 0};
#pragma unroll
    for (int ks = 0; ks < 2; ++ks)
#pragma unroll
      for (int hf = 0; hf < 2; ++hf) {
        const v4u Hb = lds_ld<v4u>(ah[hf][ks]);
#pragma unroll
        for (int cc = 0; cc < 4; ++cc) acc[cc] = mfma(R[32 + ks * 8 + hf * 4 + cc], Hb, acc[cc]);
      }
#pragma unroll
    for (int r = 0; r < 4; ++r)
      st16<SC1>((v4u){nosent(acc[0][r]), nosent(acc[1][r]), nosent(acc[2][r]), nosent(acc[3][r])}, rws, o2 + r * 2 * 8192);
    lds_stv<u32>(C_DN, 1u);
    STAMP(8);
    ENDW();
  }
  if (jw == 2) {
    __builtin_amdgcn_s_setprio(2);
    {
      const u32 tag = get_tag();
      Lander ld;
      ld.aI = L_UB + ((lane >> 3) & 3) * UB_CH + 512 + ((lane & 4) ? 4096 : 0) + (lane >> 5) * 64 + (lane & 7) * 16;
      const u32 io = WS_KX + (cg * 64 + (lane >> 5)) * 8192 + (L & 15) * 512 + (lane & 31) * 16;
#define IOFF(s_, i) (((s_) * 16 + (i) * 2) * 8192)
      ld.landed = 0;
      const u32 kfo = WS_KVF + (cg * 64 + lane) * 128, pubo = C_PUB + (lane >> 4) * 4;
      bool seen = false;
      const u32 tgate = T_KV0 + (lane >> 4) * T_KVS;
#define STAGE(s_) \
      SPIN(SPIN_L) { \
        if (((__builtin_amdgcn_ballot_w64(seen) >> (16 * s_)) & 0xFFFFull) == 0xFFFFull) break; \
        const bool act = (lds_ldv<u32>(pubo) != 0) & !seen & (now() - t0 >= tgate); \
        MEMFENCE(); \
        if (__builtin_amdgcn_ballot_w64(act) == 0ull) { ld.pending<s_>(); __builtin_amdgcn_s_sleep(1); continue; } \
        const u32 f = ld4<SC1>(rws, act ? kfo : OOB); \
        ld.pending<s_>(); \
        seen = seen | (act & (f == tag)); \
        if (((__builtin_amdgcn_ballot_w64(seen) >> (16 * s_)) & 0xFFFFull) == 0xFFFFull) break; \
        __builtin_amdgcn_s_sleep(SLP_KV); \
      } \
      MEMFENCE(); \
      STAMP(3 + s_); \
      _Pragma("unroll") for (int i = 0; i < 8; ++i) ld.in[s_][i] = ld16<SC1>(rws, io + IOFF(s_, i)); \
      SCHED();
      STAGE(0) STAGE(1) STAGE(2) STAGE(3)
#undef STAGE
      ld.pending<4>();
    }
    STAMP(7);
    lds_wait_nz(C_SEL);
    const v4u ks4 = lds_ldv<v4u>(C_SEL);
    const u32 kk = (lane & 1) ? ks4.z : ks4.y;
    const u32 frow = 2047u - (kk & 0x7FFu), kx = kk >> 16;
    const float rsel = lo2f((kx & 0x8000u) ? (kx ^ 0x8000u) : (kx ^ 0xFFFFu));
    float sig = rbf(__builtin_amdgcn_rcpf(1.f + __expf(-rsel)));
    u32 pa = WS_I2B + L * 8192 + (lane >> 1) * 128 + (lane & 1) * 16;
    u32 pb_ = lane < 48 ? pa + 4096 : OOB;
    asm volatile("" : "+v"(pa), "+v"(pb_), "+v"(sig));
    if (ffn) lds_wait_nz(C_DN); else __builtin_amdgcn_s_sleep(40);
    __builtin_amdgcn_s_sleep(4);
    v4u e0, e1;
    SPIN(SPIN_G) {
      MEMFENCE();
      e0 = ld16<SC1>(rws, pa); e1 = ld16<SC1>(rws, pb_);
      const u32 mx = umax(umax(umax(e0.x, e0.y), umax(e0.z, e0.w)), umax(umax(e1.x, e1.y), umax(e1.z, e1.w)));
      if (wave_all(mx != SENT)) break;
      __builtin_amdgcn_s_sleep(2);
    }
    STAMP(8);
    float y[4];
#pragma unroll
    for (int c = 0; c < 4; ++c) {
      const u32 ua = e0[c], ub_ = e1[c];
      float s = asf(ua) + asf(ub_);
      s += dppf<0x122, 0xf>(s); s += dppf<0x124, 0xf>(s); s += dppf<0x128, 0xf>(s);
      const auto r16 = __builtin_amdgcn_permlane16_swap(asu(s), asu(s), false, false);
      s = asf(r16[0]) + asf(r16[1]);
      const auto r32 = __builtin_amdgcn_permlane32_swap(asu(s), asu(s), false, false);
      y[c] = rbf(rbf(asf(r32[0]) + asf(r32[1])) * sig);
    }
    lds_wait_min4(C_OUT, (2047u - (umin(rfl(ks4.y) & 0x7FFu, rfl(ks4.z) & 0x7FFu))) >> 8);
    float o[4];
#pragma unroll
    for (int c = 0; c < 4; ++c) o[c] = lo2f(lds_ld<u16>(L_OS + c * OS_CH + frow * 2)) + y[c];
    st8<0>((v2u){pk2(o[0], o[1]), pk2(o[2], o[3])}, mkrs(p.out, S * D * 2), lane < 2 ? frow * 2048 + L * 8 : OOB);
    STAMP(9);
    ENDW();
  }

  const u32 tag = epoch + 1u;
  __builtin_amdgcn_s_setprio(2);
  lds_wait_min4(C_LN, 0);
  {
    const v4u r4 = lds_ld<v4u>(L_LN + (lane & 3) * 16);
    u32 a1 = umax(r4.z, r4.w), a2 = umin(r4.z, r4.w);
    u32 b1 = dppu<0xB1, 0xf>(a1), b2 = dppu<0xB1, 0xf>(a2);
    a2 = umax(umin(a1, b1), umax(a2, b2)); a1 = umax(a1, b1);
    b1 = dppu<0x4E, 0xf>(a1); b2 = dppu<0x4E, 0xf>(a2);
    a2 = umax(umin(a1, b1), umax(a2, b2)); a1 = umax(a1, b1);
    const v4u o = lane < 4 ? (v4u){r4.x, tag, r4.y, tag} : (v4u){a1, tag, a2, tag};
    st16<SC1>(o, rws, lane < 4 ? WS_REC + L * 128 + 16 + lane * 16 : lane == 4 ? WS_REC + L * 128 : OOB);
  }
  STAMP(3);
  u32 k1 = 0, k2 = 0;
  {
    u32 go[4];
#pragma unroll
    for (int i = 0; i < 4; ++i) go[i] = WS_REC + (i * 64 + lane) * 128;
    build_copies();
    hold(t0, T_REC);
    SPIN(SPIN_G) {
      MEMFENCE();
      v4u gr[4];
#pragma unroll
      for (int i = 0; i < 4; ++i) gr[i] = ld16<SC1>(rws, go[i]);
      u32 pend = OOB;
#pragma unroll
      for (int i = 0; i < 4; ++i) {
        const bool ok = (gr[i].y == tag) & (gr[i].w == tag) & (go[i] != OOB);
        const u32 ka = ok ? gr[i].x : 0u, kb = ok ? gr[i].z : 0u;
        k2 = umax(med3u(k1, k2, ka), kb); k1 = umax(k1, ka);
        go[i] = ok ? OOB : go[i];
        pend = umin(pend, go[i]);
      }
      if (wave_all(pend == OOB)) break;
      __builtin_amdgcn_s_sleep(SLP_REC);
    }
  }
  STAMP(4);
  const u32 t1 = wave_max(k1);
  const u32 t2 = wave_max(k1 == t1 ? k2 : k1);
  const u32 idx0 = 2047u - (t1 & 0x7FFu), idx1 = 2047u - (t2 & 0x7FFu);
  const u32 myrow = (lane >> 5) ? idx1 : idx0;
  const u32 so = WS_REC + (myrow >> 3) * 128 + 16 + (myrow & 7) * 8;
  const v4u xs = ld16<0>(rq, myrow * 2048 + (j & 3) * 512 + (lane & 31) * 16);
  v2u sg = ld8<SC1>(rws, so);
  v4u stt[16];
#pragma unroll
  for (int i = 0; i < 16; ++i) stt[i] = ld16<SC1>(rws, WS_REC + (i * 16 + (lane >> 2)) * 128 + 16 + (lane & 3) * 16);
  st4<SC1>(tag, rws, (L == 0 && lane == 0) ? WS_EPOCH : OOB);
  SCHED();
  if (!wave_all(sg.y == tag)) {
    SPIN(SPIN_G) {
      MEMFENCE();
      sg = ld8<SC1>(rws, so);
      if (wave_all(sg.y == tag)) break;
    }
  }
  {
    const float mu = lo2f(sg.x), rs = __builtin_amdgcn_rcpf(hi2f(sg.x));
    v4u xn;
#pragma unroll
    for (int i = 0; i < 4; ++i) { const u32 xx = xs[i]; xn[i] = pk2((lo2f(xx) - mu) * rs, (hi2f(xx) - mu) * rs); }
    lds_st<v4u>(L_XS + lane * 16, xn);
    MEMFENCE();
    lds_stv<v4u>(C_SEL, (v4u){1u, t1, t2, 0u});
    MEMFENCE();
  }
  STAMP(5);
  SPIN(SPIN_G) {
    bool ok = true;
#pragma unroll
    for (int i = 0; i < 16; ++i) ok = ok & (stt[i].y == tag) & (stt[i].w == tag);
    if (wave_all(ok)) break;
    MEMFENCE();
#pragma unroll
    for (int i = 0; i < 16; ++i) stt[i] = ld16<SC1>(rws, WS_REC + (i * 16 + (lane >> 2)) * 128 + 16 + (lane & 3) * 16);
  }
  lds_wait_min4(C_PUB, 0);
#pragma unroll
  for (int i = 0; i < 16; ++i) {
    const float r0 = __builtin_amdgcn_rcpf(hi2f(stt[i].x)), r1 = __builtin_amdgcn_rcpf(hi2f(stt[i].z));
    lds_st<f32x2>(L_RS + i * 512 + lane * 8, (f32x2){r0, r1});
    lds_st<f32x2>(L_NB + i * 512 + lane * 8, (f32x2){-lo2f(stt[i].x) * r0, -lo2f(stt[i].z) * r1});
  }
  MEMFENCE();
  lds_stv<u32>(C_STA, 1u);
  MEMFENCE();
  STAMP(6);
  {
    const rsrc_t ro = mkrs(p.out, S * D * 2);
    u32 ra = L_OS + ((lane >> 2) & 3) * OS_CH + ((lane >> 4) * 16 + (lane & 3) * 4) * 2;
    u32 row = lane;
#pragma unroll 1
    for (u32 c = 0; c < 8; ++c) {
      lds_wait_min4(C_OUT, c);
      v2u o[4];
#pragma unroll
      for (int i = 0; i < 4; ++i) o[i] = __builtin_bit_cast(v2u, __builtin_amdgcn_ds_read_tr16_b64_v4i16((LAS s16x4*)(size_t)(ra + i * 128)));
#pragma unroll
      for (int i = 0; i < 4; ++i) {
        const u32 r = row + i * 64;
        st8<0>(o[i], ro, ((r == idx0) | (r == idx1)) ? OOB : r * 2048 + L * 8);
        if (SLP_ST) { SCHED(); __builtin_amdgcn_s_sleep(SLP_ST); SCHED(); }
      }
      ra += 512; row += 256;
    }
  }
  STAMP(7);
  STAMP_FLUSH();
}

extern "C" void kernel_launch(void* const* d_in, const int* in_sizes, int n_in, void* d_out, int out_size, void* d_ws,
                              size_t ws_size, hipStream_t stream) {
  Params p;
  p.q = (const u16*)d_in[0]; p.k = (const u16*)d_in[1]; p.v = (const u16*)d_in[2];
  p.shk = (const u16*)d_in[3]; p.ssmk = (const u16*)d_in[4]; p.ssmD = (const u16*)d_in[5];
  p.wr = (const u16*)d_in[7]; p.wg = (const u16*)d_in[8]; p.wu = (const u16*)d_in[9]; p.wd = (const u16*)d_in[10];
  p.out = (u16*)d_out; p.ws = (char*)d_ws;
  (void)hipFuncSetAttribute((const void*)mega, hipFuncAttributeMaxDynamicSharedMemorySize, L_END);
  hipLaunchKernelGGL(mega, dim3(256), dim3(512), L_END, stream, p);
}
```

```cpp
#define T_REC 600
#define T_KV0 600
#define T_KVS 0
#define RW 0
#define RI 0
#define RB -1
#define RFIX 2500
#define OAUX 16
#include <hip/hip_runtime.h>
#include <stdint.h>
#include <utility>

typedef unsigned short u16;
typedef unsigned u32;
typedef unsigned long long u64;
typedef u32 v4u __attribute__((ext_vector_type(4)));
typedef u32 v2u __attribute__((ext_vector_type(2)));
typedef float f32x4 __attribute__((ext_vector_type(4)));
typedef float f32x2 __attribute__((ext_vector_type(2)));
typedef __bf16 bf16x8 __attribute__((ext_vector_type(8)));
typedef __bf16 bf16x2 __attribute__((ext_vector_type(2)));
typedef short s16x4 __attribute__((ext_vector_type(4)));
#define LAS __attribute__((address_space(3)))
#define CAS __attribute__((address_space(4)))
typedef __amdgpu_buffer_rsrc_t rsrc_t;

#define POL_IN 0
#define POL_Q 0
#define POL_W 0
#define WIN 10
#define DLY 3
#define NRES 4
#define EARLY_W 0
#define PD 4
#define CODE_KB 29
#define SC1 16
#define KXAUX 16
#define NO_W 0
#define KXL 1

constexpr int S = 2048, D = 1024, F = 3584;
constexpr u32 SENT = 0xFFFFFFFFu;
constexpr u32 OOB = 0x80000000u;
constexpr u32 SPIN_G = 1u << 16;
constexpr u32 SPIN_L = 1u << 22;

constexpr u32 WS_EPOCH = 0;
constexpr u32 WS_REC = 4096;
constexpr u32 WS_KVF = WS_REC + 32768;
constexpr u32 WS_GU = WS_KVF + 131072;
constexpr u32 WS_I2B = 1u << 20;
constexpr u32 WS_KX = 4u << 20;
constexpr u32 WS_DBG = 12u << 20;
constexpr u32 WS_END = 13u << 20;

constexpr u32 HC_COPY = 4160, HC_CH = 4 * HC_COPY;
constexpr u32 L_HC = 0;
constexpr u32 UB_CH = 512 + 4096 + 64 + 4096 + 64;
constexpr u32 L_UB = L_HC + 4 * HC_CH;
constexpr u32 OS_CH = 4224;
constexpr u32 L_OS = L_UB + 4 * UB_CH;
constexpr u32 L_RS = L_OS + 4 * OS_CH;
constexpr u32 L_NB = L_RS + 8192;
constexpr u32 L_XS = L_NB + 8192;
constexpr u32 L_HA = L_XS + 1024;
constexpr u32 L_LN = L_HA + 512;
constexpr u32 L_CTL = L_LN + 64;
constexpr u32 C_OUT = L_CTL;
constexpr u32 C_SEL = L_CTL + 16;
constexpr u32 C_LN = L_CTL + 32;
constexpr u32 C_PUB = L_CTL + 48;
constexpr u32 C_STG = L_CTL + 64;
constexpr u32 C_STA = L_CTL + 68;
constexpr u32 C_TAG = L_CTL + 72;
constexpr u32 C_ZERO = L_CTL + 80;
constexpr u32 C_DN = L_CTL + 96;
constexpr u32 C_HC = L_CTL + 112;
constexpr u32 TS_W = 8320;
constexpr u32 L_END = L_CTL + 128;

struct Params {
  const u16 *q, *k, *v, *shk, *ssmk, *ssmD, *wr, *wg, *wu, *wd;
  u16* out;
  char* ws;
};

__device__ __forceinline__ rsrc_t mkrs(const void* p, u32 bytes) {
  return __builtin_amdgcn_make_buffer_rsrc((void*)p, 0, bytes, 0x00020000);
}
template <int AUX> __device__ __forceinline__ v4u ld16(rsrc_t r, u32 off) { return __builtin_amdgcn_raw_buffer_load_b128(r, off, 0, AUX); }
template <int AUX> __device__ __forceinline__ v2u ld8(rsrc_t r, u32 off) { return __builtin_amdgcn_raw_buffer_load_b64(r, off, 0, AUX); }
template <int AUX> __device__ __forceinline__ u32 ld4(rsrc_t r, u32 off) { return __builtin_amdgcn_raw_buffer_load_b32(r, off, 0, AUX); }
template <int AUX> __device__ __forceinline__ void st16(v4u v, rsrc_t r, u32 off) { __builtin_amdgcn_raw_buffer_store_b128(v, r, off, 0, AUX); }
template <int AUX> __device__ __forceinline__ void st8(v2u v, rsrc_t r, u32 off) { __builtin_amdgcn_raw_buffer_store_b64(v, r, off, 0, AUX); }
template <int AUX> __device__ __forceinline__ void st4(u32 v, rsrc_t r, u32 off) { __builtin_amdgcn_raw_buffer_store_b32(v, r, off, 0, AUX); }

__device__ __forceinline__ float asf(u32 x) { return __builtin_bit_cast(float, x); }
__device__ __forceinline__ u32 asu(float x) { return __builtin_bit_cast(u32, x); }
__device__ __forceinline__ float lo2f(u32 x) { return asf(x << 16); }
__device__ __forceinline__ float hi2f(u32 x) { return asf(x & 0xFFFF0000u); }
__device__ __forceinline__ u32 pk2(float lo, float hi) {
  f32x2 f = {lo, hi};
  return __builtin_bit_cast(u32, __builtin_convertvector(f, bf16x2));
}
__device__ __forceinline__ float rbf(float f) { return lo2f(pk2(f, f)); }
__device__ __forceinline__ float dot2(u32 a, u32 b, float c) {
  return __builtin_amdgcn_fdot2_f32_bf16(__builtin_bit_cast(bf16x2, a), __builtin_bit_cast(bf16x2, b), c, false);
}
__device__ __forceinline__ f32x4 mfma(v4u a, v4u b, f32x4 c) {
  return __builtin_amdgcn_mfma_f32_16x16x32_bf16(__builtin_bit_cast(bf16x8, a), __builtin_bit_cast(bf16x8, b), c, 0, 0, 0);
}
#define SCHED() __builtin_amdgcn_sched_barrier(0)
#define VMCNT(n) do { SCHED(); asm volatile("s_waitcnt vmcnt(%0)" ::"n"(n) : "memory"); \
    __builtin_amdgcn_s_waitcnt(((n) & 15) | (7 << 4) | (15 << 8) | (((n) >> 4) << 14)); SCHED(); } while (0)
#define LGKM0() asm volatile("s_waitcnt lgkmcnt(0)" ::: "memory")
#define MEMFENCE() asm volatile("" ::: "memory")
#define SPIN(cap) _Pragma("unroll 1") for (u32 spins = 0; spins < (cap); ++spins)

template <int CTRL, int RM> __device__ __forceinline__ float dppf(float x) {
  return asf(__builtin_amdgcn_update_dpp(0u, asu(x), CTRL, RM, 0xf, false));
}
template <int CTRL, int RM> __device__ __forceinline__ u32 dppu(u32 x) { return __builtin_amdgcn_update_dpp(0u, x, CTRL, RM, 0xf, false); }
__device__ __forceinline__ float wave_sum63(float x) {
  x += dppf<0x111, 0xf>(x); x += dppf<0x112, 0xf>(x); x += dppf<0x114, 0xf>(x); x += dppf<0x118, 0xf>(x);
  x += dppf<0x142, 0xa>(x); x += dppf<0x143, 0xc>(x);
  return x;
}
__device__ __forceinline__ u32 umax(u32 a, u32 b) { return a > b ? a : b; }
__device__ __forceinline__ u32 umin(u32 a, u32 b) { return a < b ? a : b; }
__device__ __forceinline__ u32 wave_max(u32 x) {
  x = umax(x, dppu<0x111, 0xf>(x)); x = umax(x, dppu<0x112, 0xf>(x)); x = umax(x, dppu<0x114, 0xf>(x)); x = umax(x, dppu<0x118, 0xf>(x));
  x = umax(x, dppu<0x142, 0xa>(x)); x = umax(x, dppu<0x143, 0xc>(x));
  return __builtin_amdgcn_readlane(x, 63);
}
__device__ __forceinline__ u32 med3u(u32 a, u32 b, u32 c) {
  u32 r; asm("v_med3_u32 %0, %1, %2, %3" : "=v"(r) : "v"(a), "v"(b), "v"(c)); return r;
}
__device__ __forceinline__ bool wave_all(bool p) { return __builtin_amdgcn_ballot_w64(p) == ~0ull; }
__device__ __forceinline__ u32 nosent(float f) { return umin(asu(f), SENT - 1u); }
__device__ __forceinline__ u32 rfl(u32 x) { return __builtin_amdgcn_readfirstlane(x); }

template <typename T> __device__ __forceinline__ T lds_ld(u32 a) { return *(LAS T*)(size_t)a; }
template <typename T> __device__ __forceinline__ T lds_ldv(u32 a) { return *(volatile LAS T*)(size_t)a; }
template <typename T> __device__ __forceinline__ void lds_st(u32 a, T v) { *(LAS T*)(size_t)a = v; }
template <typename T> __device__ __forceinline__ void lds_stv(u32 a, T v) { *(volatile LAS T*)(size_t)a = v; }
__device__ __forceinline__ void lds_wait_nz(u32 a) {
  SPIN(SPIN_L) {
    if (rfl(lds_ldv<u32>(a))) break;
    __builtin_amdgcn_s_sleep(1);
  }
  MEMFENCE();
}
__device__ __forceinline__ void lds_wait_min4(u32 a, u32 gt) {
  SPIN(SPIN_L) {
    const v4u f = lds_ldv<v4u>(a);
    if (rfl(umin(umin(f.x, f.y), umin(f.z, f.w))) > gt) break;
    __builtin_amdgcn_s_sleep(1);
  }
  MEMFENCE();
}

#define STAMP(i) do { if ((i) == RI) { SCHED(); ro_t = now() - t0; SCHED(); } } while (0)
#define STAMP_DECL u32 ro_t = 0
#define STAMP_FLUSH() do { if (wave == RW && (RB < 0 || (int)blockIdx.x == RB)) { SPIN(1u << 20) { if (now() - t0 >= RFIX + 5 * ro_t) break; __builtin_amdgcn_s_sleep(1); } } } while (0)

__device__ __forceinline__ void perm8(v4u* w) {
  v4u o[8];
#pragma unroll
  for (int c = 0; c < 8; ++c)
#pragma unroll
    for (int i = 0; i < 4; ++i)
      o[c][i] = __builtin_amdgcn_perm(w[2 * i + 1][c >> 1], w[2 * i][c >> 1], (c & 1) ? 0x07060302u : 0x05040100u);
#pragma unroll
  for (int c = 0; c < 8; ++c) w[c] = o[c];
}

__device__ __forceinline__ v4u rdA(u32 aA, int m) {
  v2u lo = lds_ldv<v2u>(aA + 64 * m), hi = lds_ldv<v2u>(aA + 64 * m + 8);
  return (v4u){lo.x, lo.y, hi.x, hi.y};
}

constexpr int NSTEP = 160, RING = PD + 2;
constexpr int st_pp(int s) { return s < 16 ? 0 : s < 48 ? 1 : s < 96 ? 2 : 3; }
constexpr int st_b0(int pp) { return pp == 0 ? 0 : pp == 1 ? 16 : pp == 2 ? 48 : 96; }
constexpr int st_ls(int s) { return s - st_b0(st_pp(s)); }
constexpr bool st_diag(int s) { return st_ls(s) >= 16 * st_pp(s); }
constexpr int st_m0(int s) { return st_diag(s) ? (st_ls(s) - 16 * st_pp(s)) / 2 : st_ls(s) / (2 * st_pp(s)); }
constexpr int st_bb(int s) { return st_diag(s) ? 2 * st_pp(s) + (st_ls(s) & 1) : st_ls(s) % (2 * st_pp(s)); }
constexpr bool st_needP(int s) { return s < NSTEP && st_bb(s) == 0 && 2 * st_pp(s) + 1 >= NRES; }
constexpr bool st_needA(int s) { return s < NSTEP && 2 * st_pp(s) - st_bb(s) >= NRES; }
constexpr bool st_light(int s) { return !st_needA(s + PD) && !st_needP(s + PD + 1); }
constexpr int FIN0 = 4;
constexpr int st_piece(int s) {
  if (st_pp(s) == 0 || st_ls(s) < FIN0 || !st_light(s)) return -1;
  int n = 0;
  for (int t = st_b0(st_pp(s)) + FIN0; t < s; ++t) n += st_light(t) ? 1 : 0;
  return n < 10 ? n : -1;
}

__device__ __forceinline__ void mfma_acc(f32x4& c, v4u a, v4u b) {
  asm volatile("v_mfma_f32_16x16x32_bf16 %0, %1, %2, %0" : "+v"(c) : "v"(a), "v"(b));
}
__device__ __forceinline__ void mfma_one(f32x4& c, v4u a, v4u b) {
  asm volatile("v_mfma_f32_16x16x32_bf16 %0, %1, %2, 1.0" : "=&v"(c) : "v"(a), "v"(b));
}

struct Conv {
  v4u A[8 * NRES], AD, Bq[RING];
  v2u Al[RING], Ah[RING], Pl[2], Ph[2];
  f32x4 acc[2][2], rs[2], nb[2];
  float o[4];
  v2u xx[2];
  u32 aA, aB, aX, aS, osw, flg, pk;

  template <int s> __device__ __forceinline__ void rdB() {
    if constexpr (s < NSTEP) Bq[s % RING] = lds_ldv<v4u>(aB + (8 * st_bb(s) - st_m0(s) + 7) * 64);
  }
  template <int s, int h> __device__ __forceinline__ void rdAq() {
    if constexpr (st_needA(s)) {
      constexpr int m = st_m0(s) + 8 * (2 * st_pp(s) - st_bb(s));
      if constexpr (h == 0) Al[s % RING] = lds_ldv<v2u>(aA + 64 * m); else Ah[s % RING] = lds_ldv<v2u>(aA + 64 * m + 8);
    }
  }
  template <int s, int h> __device__ __forceinline__ void rdP() {
    if constexpr (st_needP(s)) {
      constexpr int m = st_m0(s) + 8 * (2 * st_pp(s) + 1);
      if constexpr (h == 0) Pl[st_m0(s) & 1] = lds_ldv<v2u>(aA + 64 * m); else Ph[st_m0(s) & 1] = lds_ldv<v2u>(aA + 64 * m + 8);
    }
  }
  template <int s> __device__ __forceinline__ v4u ringA() {
    const v2u l = Al[s % RING], h = Ah[s % RING];
    return (v4u){l.x, l.y, h.x, h.y};
  }
  template <int pp> __device__ __forceinline__ void fin_load() {
#pragma unroll
    for (int e = 0; e < 2; ++e) {
      xx[e] = lds_ldv<v2u>(aX + (2 * pp + e) * 512);
      rs[e] = lds_ldv<f32x4>(aS + (2 * pp + e) * 1024);
      nb[e] = lds_ldv<f32x4>(aS + (2 * pp + e) * 1024 + 8192);
    }
  }
  template <int pp, int pc> __device__ __forceinline__ void piece() {
    constexpr int e = pc / 5, r = pc % 5;
    if constexpr (r < 4) {
      const u32 xw = xx[e][r >> 1];
      const float x = (r & 1) ? hi2f(xw) : lo2f(xw);
      o[r] = __builtin_fmaf(__builtin_fmaf(x, rs[e][r], nb[e][r]), acc[pp & 1][e][r], x);
      asm volatile("" : "+v"(o[r]));
    } else {
      lds_stv<v2u>(osw + (2 * pp + e) * 512, (v2u){pk2(o[0], o[1]), pk2(o[2], o[3])});
      lds_stv<u32>(flg, 2 * pp + e + 1);
    }
  }
  template <int s> __device__ __forceinline__ void step() {
    constexpr int pp = st_pp(s), m0 = st_m0(s), bb = st_bb(s), c0 = 2 * pp, c1 = c0 + 1, ls = st_ls(s);
    if constexpr (pp > 0 && ls == 16 * pp - PD - 3) pk = lds_ldv<u32>(C_STG);
    if constexpr (pp > 0 && ls == 16 * pp - PD) {
      if (rfl(pk) <= (u32)pp) {
        SPIN(SPIN_L) { if (rfl(lds_ldv<u32>(C_STG)) > (u32)pp) break; __builtin_amdgcn_s_sleep(1); }
      }
    }
    if constexpr (s == 13) pk = lds_ldv<u32>(C_STA);
    if constexpr (s == 16) { if (rfl(pk) == 0) lds_wait_nz(C_STA); }
    const v4u Bf = Bq[s % RING];
    v4u A1, A0;
    if constexpr (c1 - bb < NRES) A1 = A[m0 + 8 * (c1 - bb)];
    else if constexpr (bb == 0) A1 = (v4u){Pl[m0 & 1].x, Pl[m0 & 1].y, Ph[m0 & 1].x, Ph[m0 & 1].y};
    else A1 = ringA<s - 1>();
    if constexpr (bb <= c0) {
      if constexpr (c0 - bb < NRES) A0 = A[m0 + 8 * (c0 - bb)]; else A0 = ringA<s>();
    }
    if constexpr (st_needA(s)) asm volatile("" :: "v"(Ah[s % RING]));
    if constexpr (ls == 0) mfma_one(acc[pp & 1][1], A1, Bf); else mfma_acc(acc[pp & 1][1], A1, Bf);
    rdB<s + PD>();
    rdAq<s + PD, 0>();
    rdP<s + PD + 1, 0>();
    SCHED();
    if constexpr (bb <= c0) {
      if constexpr (ls == 0) mfma_one(acc[pp & 1][0], A0, Bf); else mfma_acc(acc[pp & 1][0], A0, Bf);
    }
    rdAq<s + PD, 1>();
    rdP<s + PD + 1, 1>();
    if constexpr (st_piece(s) >= 0) piece<pp - 1, st_piece(s)>();
    if constexpr (m0 == 0 && bb == c1) mfma_acc(acc[pp & 1][1], AD, Bf);
    if constexpr (m0 == 0 && bb == c0) mfma_acc(acc[pp & 1][0], AD, Bf);
    if constexpr (pp > 0 && ls == 0) fin_load<pp - 1>();
    if constexpr (s == NSTEP - 4) fin_load<3>();
    SCHED();
  }
  template <int... Is> __device__ __forceinline__ void pre(std::integer_sequence<int, Is...>) { (rdB<Is>(), ...); }
  template <int... Is> __device__ __forceinline__ void run(std::integer_sequence<int, Is...>) { (step<Is>(), ...); }
  template <int... Is> __device__ __forceinline__ void last(std::integer_sequence<int, Is...>) { (piece<3, Is>(), ...); }
  __device__ __forceinline__ void stream() {
    pre(std::make_integer_sequence<int, PD>{});
    run(std::make_integer_sequence<int, NSTEP>{});
    asm volatile("s_nop 7" : "+v"(acc[1][0]), "+v"(acc[1][1]));
    last(std::make_integer_sequence<int, 10>{});
  }
  __device__ __forceinline__ void setup(int w, int lane, u32 Dbits) {
    const int M = lane & 15, q4 = lane >> 4;
    const u32 ub = L_UB + w * UB_CH, hb = L_HC + w * HC_CH;
    aA = hb + ((M + 1) & 3) * HC_COPY + ((M + 1) >> 2) * 8 + q4 * 16;
#pragma unroll
    for (int m = 0; m < 8 * NRES; ++m) A[m] = rdA(aA, m);
    const int jd = 15 - M - 8 * q4;
#pragma unroll
    for (int i = 0; i < 4; ++i) AD[i] = jd == 2 * i ? Dbits : jd == 2 * i + 1 ? (Dbits << 16) : 0u;
    aB = ub + 512 - 7 * 64 + (2 * M + 1 - q4) * 16;
    aX = ub + 512 + 4096 + 64 + (16 * M + 4 * q4) * 2;
    aS = L_RS + (16 * M + 4 * q4) * 4;
    osw = L_OS + w * OS_CH + (16 * M + 4 * q4) * 2;
    flg = C_OUT + w * 4;
    asm volatile("" : "+v"(aB), "+v"(aA), "+v"(aX), "+v"(aS), "+v"(flg), "+v"(osw), "+v"(AD));
#pragma unroll
    for (int m = 0; m < 8 * NRES; ++m) asm volatile("" : "+v"(A[m]));
  }
};

#define T_W 0
#define SLP_ST 2
#define SLP_REC 4
#define SLP_KV 2

__device__ __forceinline__ u32 get_tag() {
  u32 tag = 0;
  SPIN(SPIN_L) {
    const v2u f = lds_ldv<v2u>(C_TAG);
    tag = rfl(f.y);
    if (rfl(f.x)) break;
    __builtin_amdgcn_s_sleep(1);
  }
  return tag;
}

struct Lander {
  v4u in[4][8];
  u32 aI, landed;
  template <int s> __device__ __forceinline__ void pending() {
#pragma unroll
    for (int t = 0; t < s; ++t)
      if (landed == (u32)t) {
#pragma unroll
        for (int i = 0; i < 8; ++i) lds_st<v4u>(aI + t * 1024 + i * 128, in[t][i]);
        MEMFENCE();
        lds_stv<u32>(C_STG, t + 1);
        MEMFENCE();
        landed = t + 1;
      }
  }
};

__device__ __forceinline__ u32 now() { return (u32)__builtin_amdgcn_s_memrealtime(); }
__device__ __forceinline__ void hold(u32 t0, u32 dt) {
  SPIN(4096) { if (now() - t0 >= dt) break; __builtin_amdgcn_s_sleep(1); }
}
template <int AUX> __device__ __forceinline__ v4u ld16s(rsrc_t r, u32 off, u32 soff) { return __builtin_amdgcn_raw_buffer_load_b128(r, off, soff, AUX); }

typedef u32 v16u __attribute__((ext_vector_type(16)));
typedef u32 v8u __attribute__((ext_vector_type(8)));
#define ENDW() do { STAMP_FLUSH(); asm volatile("s_endpgm" : "=v"(T[0]), "=v"(T[1]), "=v"(T[2]), "=v"(T[3]), "=v"(T[4]), "=v"(T[5]), \
    "=v"(T[6]), "=v"(T[7]), "=v"(T[8]), "=v"(T[9]), "=v"(T[10]), "=v"(T[11]), "=v"(hn[0]), "=v"(hn[1]), "=v"(hn[2]), "=v"(hn[3]) :: "memory"); } while (0)

__global__ void __launch_bounds__(512, 2) mega(Params p_unused) {
  extern __shared__ __attribute__((aligned(16))) char smem[];
  u64 pc0;
  v16u ka;
  v8u kb;
  asm volatile("s_load_dwordx16 %1, %3, 0x0\n\ts_load_dwordx8 %2, %3, 0x40\n\ts_getpc_b64 %0"
               : "=&s"(pc0), "=&s"(ka), "=&s"(kb) : "s"(__builtin_amdgcn_kernarg_segment_ptr()));
  const int tid = threadIdx.x;
  const int wave = __builtin_amdgcn_readfirstlane(tid >> 6);
  const int lane = tid & 63;
  const int b = blockIdx.x, xcd = b & 7, rk = b >> 3;
  const u32 codepf = ld4<0>(mkrs((const void*)(pc0 & ~255ull), CODE_KB * 1024), lane < 8 ? (rk & 3) * 8192 + wave * 1024 + lane * 128 : OOB);
  SCHED();
  const u32 t0 = now();
  const int L = xcd * 32 + rk;
  STAMP_DECL;
  STAMP(0);
  lds_st<v4u>(L_CTL + (tid & 7) * 16, (v4u){0, 0, 0, 0});
  const int jw = wave & 3, cg = L >> 4, rb = L & 15, ch = 4 * L + jw;
  const bool isconv = wave < 4;
  const u32 toff = isconv ? (u32)((jw * 512 + rb * 32 + (lane >> 3) * 4) * 2048 + cg * 128 + (lane & 7) * 16) : (u32)(lane * 16);
  const bool ffn = rk < 28;
  const int j = xcd * 28 + rk;
  u32 c0off = ((wave >> 1) == 2 && ffn) ? WS_GU + j * 1024 + jw * 512 + lane * 16 + (lane >> 5) * OOB
              : wave == 6 ? WS_I2B + L * 8192 + (lane >> 1) * 128 + (lane & 1) * 16 : OOB;
  u32 c1off = (wave == 6 && lane < 48) ? c0off + 4096 : OOB;
  asm volatile("" : "+v"(c0off), "+v"(c1off));
  asm volatile("s_waitcnt lgkmcnt(0)" : "+s"(ka), "+s"(kb) :: "memory");
  __builtin_amdgcn_s_barrier();
  SCHED();
  Params p;
#define KP(v, i) (((u64)v[2 * (i) + 1] << 32) | v[2 * (i)])
  p.q = (const u16*)KP(ka, 0); p.k = (const u16*)KP(ka, 1); p.v = (const u16*)KP(ka, 2); p.shk = (const u16*)KP(ka, 3);
  p.ssmk = (const u16*)KP(ka, 4); p.ssmD = (const u16*)KP(ka, 5); p.wr = (const u16*)KP(ka, 6); p.wg = (const u16*)KP(ka, 7);
  p.wu = (const u16*)KP(kb, 0); p.wd = (const u16*)KP(kb, 1); p.out = (u16*)KP(kb, 2); p.ws = (char*)KP(kb, 3);
#undef KP
  const rsrc_t rws = mkrs(p.ws, WS_END);
  v2u hn[4];
  v4u T[12];
  {
    const rsrc_t r1 = mkrs(isconv ? p.k : p.q + (8 * L + 2 * jw) * D, S * D * 2);
    const rsrc_t r2 = mkrs(isconv ? p.v : p.wr, isconv ? S * D * 2 : D * 2);
    const rsrc_t r3 = mkrs(isconv ? p.q : p.ssmk + ch * S, S * D * 2);
    const u32 str = isconv ? 2048 : 1024;
    for (int i = 0; i < (isconv ? jw : 0); ++i) __builtin_amdgcn_s_sleep(DLY);
    SCHED();
#pragma unroll
    for (int i = 0; i < 4; ++i) T[i] = ld16s<POL_IN>(r1, toff, i * str);
#pragma unroll
    for (int i = 0; i < 4; ++i) T[4 + i] = ld16s<POL_IN>(r2, toff, i * str);
    SCHED();
    st16<SC1>((v4u){SENT, SENT, SENT, SENT}, rws, c0off);
    st16<SC1>((v4u){SENT, SENT, SENT, SENT}, rws, c1off);
    SCHED();
#pragma unroll
    for (int i = 0; i < 4; ++i) T[8 + i] = ld16s<POL_IN>(r3, toff, i * str);
    SCHED();
  }

  if (isconv) {
    v4u R[19];
    const int w = jw;
    const int g = lane & 7, rr = lane >> 3;
    const rsrc_t rk_ = mkrs(p.k, S * D * 2), rs = mkrs(p.shk, D * 8);
#pragma unroll
    for (int i = 0; i < 3; ++i) R[i] = ld16<0>(rk_, toff - (3 - i) * 2048);
#pragma unroll
    for (int i = 0; i < 4; ++i) R[11 + i] = ld16<0>(rs, (cg * 64 + g * 8) * 8 + i * 16);
    const u32 Dw = *(const CAS u32*)(p.ssmD + (ch & ~1));
    SCHED();
#pragma unroll
    for (int i = 0; i < 4; ++i) { R[3 + i] = T[i]; R[7 + i] = T[4 + i]; R[15 + i] = T[8 + i]; }
    const u32 hb = L_OS + w * TS_W;
    if (lane < 32) lds_st<v4u>(L_UB + w * UB_CH + lane * 16, (v4u){0, 0, 0, 0});
    STAMP(1);
    VMCNT(0);
    asm volatile("" :: "v"(codepf));
    STAMP(2);
    const u32 tw = hb + g * 1040 + (rr >> 1) * 16;
#pragma unroll
    for (int c = 0; c < 8; ++c) {
      const u32 wa = R[11 + (c >> 1)][(c & 1) * 2], wb = R[11 + (c >> 1)][(c & 1) * 2 + 1];
      const u32 sel = (c & 1) ? 0x07060302u : 0x05040100u;
      u32 P[6];
      float kv[4];
#pragma unroll
      for (int i = 0; i < 6; ++i) P[i] = __builtin_amdgcn_perm(R[i][c >> 1], R[i + 1][c >> 1], sel);
#pragma unroll
      for (int i = 0; i < 4; ++i) {
        const u32 vx = R[7 + i][c >> 1];
        kv[i] = dot2(P[i + 2], wa, dot2(P[i], wb, 0.f)) * ((c & 1) ? hi2f(vx) : lo2f(vx));
      }
      lds_st<v2u>(tw + c * 128 + ((rr & 1) ? 0 : 8), (v2u){pk2(kv[3], kv[2]), pk2(kv[1], kv[0])});
    }
    SCHED();
#pragma unroll
    for (int c = 0; c < 8; ++c) {
      const u32 sel = (c & 1) ? 0x07060302u : 0x05040100u;
      lds_st<v2u>(tw + c * 128 + 64 + ((rr & 1) ? 8 : 0),
                  (v2u){__builtin_amdgcn_perm(R[16][c >> 1], R[15][c >> 1], sel), __builtin_amdgcn_perm(R[18][c >> 1], R[17][c >> 1], sel)});
    }
    MEMFENCE();
#pragma unroll
    for (int i = 0; i < 8; ++i) {
      v4u t = lds_ld<v4u>(hb + i * 1040 + lane * 16);
      st16<KXAUX>(t, rws, WS_KX + (cg * 64 + w * 16 + rb) * 8192 + i * 1024 + lane * 16);
    }
    STAMP(3);
    const u32 tag = get_tag();
    VMCNT(0);
    st4<SC1>(tag, rws, lane == 0 ? WS_KVF + (cg * 64 + w * 16 + rb) * 128 : OOB);
    lds_stv<u32>(C_PUB + w * 4, 1u);
    STAMP(4);
    SCHED();
    lds_wait_nz(C_HC + w * 4);
    Conv cv;
    cv.setup(w, lane, (ch & 1) ? (Dw >> 16) : (Dw & 0xFFFFu));
    STAMP(5);
    lds_wait_nz(C_STG);
    STAMP(6);
    cv.stream();
    STAMP(7);
    ENDW();
  }

  const rsrc_t rq = mkrs(p.q, S * D * 2);
  v4u R[48];
  u32 epoch = 0;
  {
    if (jw == 3) epoch = *(const CAS u32*)(p.ws + WS_EPOCH);
    const rsrc_t rh = mkrs(p.ssmk, S * D * 2);
#pragma unroll
    for (int i = 0; i < 4; ++i) hn[i] = ld8<0>(rh, ch * 4096 + (i * 64 + lane) * 16 + 16);
    SCHED();
  }
  const rsrc_t rw = mkrs(jw == 0 ? p.wg : p.wu, (ffn && jw < 2 && !NO_W) ? D * F * 2 : 0);
  const u32 wo = ((j & 3) * 256 + (lane >> 3) * 8) * (F * 2) + (j >> 2) * 128 + (lane & 7) * 16;
#pragma unroll
  for (int i = 0; i < EARLY_W; ++i) R[i] = ld16<POL_W>(rw, wo + ((i >> 3) * 64 + (i & 7)) * (F * 2));
  SCHED();
  asm volatile("" : "+s"(epoch));
  if (jw == 3) lds_stv<v2u>(C_TAG, (v2u){1u, epoch + 1u});
  STAMP(1);
  {
    float sa = 0.f, sb = 0.f, qa = 0.f, qb = 0.f, ra = 0.f, rb_ = 0.f, sw = 0.f;
#pragma unroll
    for (int i = 0; i < 8; ++i) {
      const u32 a = T[i >> 2][i & 3], bq = T[2 + (i >> 2)][i & 3], wv = T[4 + (i >> 2)][i & 3];
      sa = dot2(a, 0x3f803f80u, sa); sb = dot2(bq, 0x3f803f80u, sb);
      qa = dot2(a, a, qa); qb = dot2(bq, bq, qb);
      ra = dot2(a, wv, ra); rb_ = dot2(bq, wv, rb_);
      sw = dot2(wv, 0x3f803f80u, sw);
    }
    sa = wave_sum63(sa); sb = wave_sum63(sb); qa = wave_sum63(qa); qb = wave_sum63(qb);
    ra = wave_sum63(ra); rb_ = wave_sum63(rb_); sw = wave_sum63(sw);
    const float ma = sa * (1.f / D), mb = sb * (1.f / D);
    const float va = __builtin_fmaxf(__builtin_fmaf(-ma, ma, qa * (1.f / D)), 0.f), vb = __builtin_fmaxf(__builtin_fmaf(-mb, mb, qb * (1.f / D)), 0.f);
    const u32 mm = pk2(ma, mb), v1 = pk2(va, vb);
    const u32 v2 = pk2(lo2f(v1) + 1e-5f, hi2f(v1) + 1e-5f);
    const u32 sd = pk2(__builtin_amdgcn_sqrtf(lo2f(v2)), __builtin_amdgcn_sqrtf(hi2f(v2)));
    u32 rp = pk2(__builtin_fmaf(-lo2f(mm), sw, ra) * __builtin_amdgcn_rcpf(lo2f(sd)),
                 __builtin_fmaf(-hi2f(mm), sw, rb_) * __builtin_amdgcn_rcpf(hi2f(sd)));
    const u32 neg = ((rp >> 15) & 0x00010001u) * 0xFFFFu;
    rp = rp ^ (neg | 0x80008000u);
    const u32 inv = 2047u - (u32)(8 * L + 2 * jw);
    v4u rec = {(mm & 0xFFFFu) | (sd << 16), (mm >> 16) | (sd & 0xFFFF0000u), (rp << 16) | inv, (rp & 0xFFFF0000u) | (inv - 1u)};
    asm volatile("" : "+v"(rec) : "v"(codepf));
    VMCNT(8 + EARLY_W);
    if (lane == 63) {
      lds_st<v4u>(L_LN + jw * 16, rec);
      MEMFENCE();
      lds_stv<u32>(C_LN + jw * 4, 1u);
    }
    MEMFENCE();
  }
  STAMP(2);
  auto build_copies = [&]() {
    const u32 hb = L_HC + jw * HC_CH;
#pragma unroll
    for (int i = 0; i < 4; ++i) {
      const v4u d = T[8 + i];
      const u32 e0 = __builtin_amdgcn_alignbit(d.y, d.x, 16), e1 = __builtin_amdgcn_alignbit(d.z, d.y, 16),
                e2 = __builtin_amdgcn_alignbit(d.w, d.z, 16), e3 = __builtin_amdgcn_alignbit(hn[i].x, d.w, 16),
                e4 = __builtin_amdgcn_alignbit(hn[i].y, hn[i].x, 16);
      const u32 a = hb + 32 + (i * 64 + lane) * 16;
      lds_st<v4u>(a, d);
      lds_st<v4u>(a + HC_COPY, (v4u){e0, e1, e2, e3});
      lds_st<v4u>(a + 2 * HC_COPY, (v4u){d.y, d.z, d.w, hn[i].x});
      lds_st<v4u>(a + 3 * HC_COPY, (v4u){e1, e2, e3, e4});
    }
    {
      const u32 s0 = rfl(T[8].x), s1 = rfl(T[8].y);
      const int cb = lane & 3;
      const u32 w3 = cb == 0 ? 0u : cb == 1 ? (s0 << 16) : cb == 2 ? s0 : ((s0 >> 16) | (s1 << 16));
      const v4u z = {0, 0, lane == 7 ? (s0 << 16) : 0u, (lane & 4) ? w3 : 0u};
      if (lane < 8) lds_st<v4u>(hb + cb * HC_COPY + (lane >> 2) * 16, z);
    }
    MEMFENCE();
    SCHED();
    lds_stv<u32>(C_HC + jw * 4, 1u);
    MEMFENCE();
    SCHED();
  };
  if (jw < 3) build_copies();

  if (jw < 2) {
    if (!ffn) ENDW();
    const int q4 = lane >> 4, N = lane & 15;
    const rsrc_t rd = mkrs(p.wd, NO_W ? 0 : D * F * 2);
    const u32 dofs = ((j >> 2) * 64 + q4 * 8) * 2048 + (j & 3) * 512 + jw * 256 + N * 16;
    if (T_W) hold(t0, T_W);
#pragma unroll
    for (int i = EARLY_W; i < 48; ++i) {
      if (i >= WIN && i < 32 + WIN) VMCNT(WIN - 1);
      if (i < 32) R[i] = ld16<POL_W>(rw, wo + ((i >> 3) * 64 + (i & 7)) * (F * 2));
      else R[i] = ld16<POL_W>(rd, dofs + (((i >> 3) & 1) * 32 + (i & 7)) * 2048);
      SCHED();
      if (i >= WIN + 8 && ((i - WIN) & 7) == 0 && i - WIN <= 24) { perm8(&R[i - WIN - 8]); SCHED(); }
    }
    static_assert(WIN <= 16 && WIN >= EARLY_W, "");
    perm8(&R[24]);
#pragma unroll
    for (int i = 0; i < 32; ++i) asm volatile("" : "+v"(R[i]));
    u32 ax = L_XS + ((lane >> 1) & 1) * 512 + q4 * 32 + (lane & 1) * 16;
    const bool kap = (lane >> 3) & 1;
    u32 oo = lane < 16 ? WS_GU + j * 1024 + jw * 512 + (lane & 7) * 64 + (kap ? 32 : 0) : OOB;
    asm volatile("" : "+v"(ax), "+v"(oo));
    STAMP(3);
    lds_wait_nz(C_SEL);
    __builtin_amdgcn_s_setprio(2);
    STAMP(4);
    {
      f32x4 acc[8];
#pragma unroll
      for (int c = 0; c < 8; ++c) acc[c] = (f32x4){0, 0, 0, 0};
#pragma unroll
      for (int bk = 0; bk < 4; ++bk) {
        const v4u X = lds_ld<v4u>(ax + bk * 128);
#pragma unroll
        for (int c = 0; c < 8; ++c) acc[c] = mfma(X, R[bk * 8 + c], acc[c]);
      }
      u32 o[8];
#pragma unroll
      for (int cc = 0; cc < 4; ++cc)
#pragma unroll
        for (int t = 0; t < 2; ++t) {
          const float keep = kap ? acc[4 + cc][2 * t + 1] : acc[cc][2 * t], send = kap ? acc[cc][2 * t + 1] : acc[4 + cc][2 * t];
          o[cc * 2 + t] = nosent(keep + dppf<0x128, 0xf>(send));
        }
      st16<SC1>((v4u){o[0], o[1], o[2], o[3]}, rws, oo);
      st16<SC1>((v4u){o[4], o[5], o[6], o[7]}, rws, oo + 16);
    }
    STAMP(5);
    SCHED();
    perm8(&R[32]);
    perm8(&R[40]);
#pragma unroll
    for (int i = 32; i < 48; ++i) asm volatile("" : "+v"(R[i]));
    const u32 ha = L_HA + jw * 256;
    u32 ah[2][2];
#pragma unroll
    for (int hf = 0; hf < 2; ++hf)
#pragma unroll
      for (int ks = 0; ks < 2; ++ks) {
        ah[hf][ks] = (N >> 1) == hf ? ha + (N & 1) * 128 + ks * 64 + q4 * 16 : C_ZERO;
        asm volatile("" : "+v"(ah[hf][ks]));
      }
    u32 o2 = N < 4 ? WS_I2B + ((j & 3) * 64 + jw * 32 + q4 * 8 + (N >> 1)) * 8192 + (j >> 2) * 128 + (N & 1) * 16 : OOB;
    u32 po = WS_GU + (j & ~3) * 1024 + lane * 8;
    asm volatile("" : "+v"(o2), "+v"(po));
    STAMP(6);
    v2u pv[8];
    SPIN(SPIN_G) {
      MEMFENCE();
      u32 mx = 0;
#pragma unroll
      for (int i = 0; i < 8; ++i) pv[i] = ld8<SC1>(rws, po + i * 512);
#pragma unroll
      for (int i = 0; i < 8; ++i) mx = umax(mx, umax(pv[i].x, pv[i].y));
      if (wave_all(mx != SENT)) break;
      __builtin_amdgcn_s_sleep(2);
    }
    STAMP(7);
    const float g0 = (asf(pv[0].x) + asf(pv[2].x)) + (asf(pv[4].x) + asf(pv[6].x)), g1 = (asf(pv[0].y) + asf(pv[2].y)) + (asf(pv[4].y) + asf(pv[6].y));
    const float u0 = (asf(pv[1].x) + asf(pv[3].x)) + (asf(pv[5].x) + asf(pv[7].x)), u1 = (asf(pv[1].y) + asf(pv[3].y)) + (asf(pv[5].y) + asf(pv[7].y));
    const u32 gp = pk2(g0, g1), up = pk2(u0, u1);
    const float ga = lo2f(gp), gb = hi2f(gp);
    const u32 sp = pk2(ga * __builtin_amdgcn_rcpf(1.f + __expf(-ga)), gb * __builtin_amdgcn_rcpf(1.f + __expf(-gb)));
    const u32 hp = pk2(lo2f(sp) * lo2f(up), hi2f(sp) * hi2f(up));
    lds_st<u16>(ha + lane * 2, (u16)hp);
    lds_st<u16>(ha + 128 + lane * 2, (u16)(hp >> 16));
    MEMFENCE();
    f32x4 acc[4];
#pragma unroll
    for (int c = 0; c < 4; ++c) acc[c] = (f32x4){0, 0, 0, 0};
#pragma unroll
    for (int ks = 0; ks < 2; ++ks)
#pragma unroll
      for (int hf = 0; hf < 2; ++hf) {
        const v4u Hb = lds_ld<v4u>(ah[hf][ks]);
#pragma unroll
        for (int cc = 0; cc < 4; ++cc) acc[cc] = mfma(R[32 + ks * 8 + hf * 4 + cc], Hb, acc[cc]);
      }
#pragma unroll
    for (int r = 0; r < 4; ++r)
      st16<SC1>((v4u){nosent(acc[0][r]), nosent(acc[1][r]), nosent(acc[2][r]), nosent(acc[3][r])}, rws, o2 + r * 2 * 8192);
    lds_stv<u32>(C_DN, 1u);
    STAMP(8);
    ENDW();
  }
  if (jw == 2) {
    __builtin_amdgcn_s_setprio(2);
    {
      const u32 tag = get_tag();
      Lander ld;
      ld.aI = L_UB + ((lane >> 3) & 3) * UB_CH + 512 + ((lane & 4) ? 4096 : 0) + (lane >> 5) * 64 + (lane & 7) * 16;
      const u32 io = WS_KX + (cg * 64 + (lane >> 5)) * 8192 + (L & 15) * 512 + (lane & 31) * 16;
#define IOFF(s_, i) (((s_) * 16 + (i) * 2) * 8192)
      ld.landed = 0;
      const u32 kfo = WS_KVF + (cg * 64 + lane) * 128, pubo = C_PUB + (lane >> 4) * 4;
      bool seen = false;
      const u32 tgate = T_KV0 + (lane >> 4) * T_KVS;
#define STAGE(s_) \
      SPIN(SPIN_L) { \
        if (((__builtin_amdgcn_ballot_w64(seen) >> (16 * s_)) & 0xFFFFull) == 0xFFFFull) break; \
        const bool act = (lds_ldv<u32>(pubo) != 0) & !seen & (now() - t0 >= tgate); \
        MEMFENCE(); \
        if (__builtin_amdgcn_ballot_w64(act) == 0ull) { ld.pending<s_>(); __builtin_amdgcn_s_sleep(1); continue; } \
        const u32 f = ld4<SC1>(rws, act ? kfo : OOB); \
        ld.pending<s_>(); \
        seen = seen | (act & (f == tag)); \
        if (((__builtin_amdgcn_ballot_w64(seen) >> (16 * s_)) & 0xFFFFull) == 0xFFFFull) break; \
        __builtin_amdgcn_s_sleep(SLP_KV); \
      } \
      MEMFENCE(); \
      STAMP(3 + s_); \
      _Pragma("unroll") for (int i = 0; i < 8; ++i) ld.in[s_][i] = ld16<SC1>(rws, io + IOFF(s_, i)); \
      SCHED();
      STAGE(0) STAGE(1) STAGE(2) STAGE(3)
#undef STAGE
      ld.pending<4>();
    }
    STAMP(7);
    lds_wait_nz(C_SEL);
    const v4u ks4 = lds_ldv<v4u>(C_SEL);
    const u32 kk = (lane & 1) ? ks4.z : ks4.y;
    const u32 frow = 2047u - (kk & 0x7FFu), kx = kk >> 16;
    const float rsel = lo2f((kx & 0x8000u) ? (kx ^ 0x8000u) : (kx ^ 0xFFFFu));
    float sig = rbf(__builtin_amdgcn_rcpf(1.f + __expf(-rsel)));
    u32 pa = WS_I2B + L * 8192 + (lane >> 1) * 128 + (lane & 1) * 16;
    u32 pb_ = lane < 48 ? pa + 4096 : OOB;
    asm volatile("" : "+v"(pa), "+v"(pb_), "+v"(sig));
    if (ffn) lds_wait_nz(C_DN); else __builtin_amdgcn_s_sleep(40);
    __builtin_amdgcn_s_sleep(4);
    v4u e0, e1;
    SPIN(SPIN_G) {
      MEMFENCE();
      e0 = ld16<SC1>(rws, pa); e1 = ld16<SC1>(rws, pb_);
      const u32 mx = umax(umax(umax(e0.x, e0.y), umax(e0.z, e0.w)), umax(umax(e1.x, e1.y), umax(e1.z, e1.w)));
      if (wave_all(mx != SENT)) break;
      __builtin_amdgcn_s_sleep(2);
    }
    STAMP(8);
    float y[4];
#pragma unroll
    for (int c = 0; c < 4; ++c) {
      const u32 ua = e0[c], ub_ = e1[c];
      float s = asf(ua) + asf(ub_);
      s += dppf<0x122, 0xf>(s); s += dppf<0x124, 0xf>(s); s += dppf<0x128, 0xf>(s);
      const auto r16 = __builtin_amdgcn_permlane16_swap(asu(s), asu(s), false, false);
      s = asf(r16[0]) + asf(r16[1]);
      const auto r32 = __builtin_amdgcn_permlane32_swap(asu(s), asu(s), false, false);
      y[c] = rbf(rbf(asf(r32[0]) + asf(r32[1])) * sig);
    }
    lds_wait_min4(C_OUT, (2047u - (umin(rfl(ks4.y) & 0x7FFu, rfl(ks4.z) & 0x7FFu))) >> 8);
    float o[4];
#pragma unroll
    for (int c = 0; c < 4; ++c) o[c] = lo2f(lds_ld<u16>(L_OS + c * OS_CH + frow * 2)) + y[c];
    st8<OAUX>((v2u){pk2(o[0], o[1]), pk2(o[2], o[3])}, mkrs(p.out, S * D * 2), lane < 2 ? frow * 2048 + L * 8 : OOB);
    STAMP(9);
    ENDW();
  }

  const u32 tag = epoch + 1u;
  __builtin_amdgcn_s_setprio(2);
  lds_wait_min4(C_LN, 0);
  {
    const v4u r4 = lds_ld<v4u>(L_LN + (lane & 3) * 16);
    u32 a1 = umax(r4.z, r4.w), a2 = umin(r4.z, r4.w);
    u32 b1 = dppu<0xB1, 0xf>(a1), b2 = dppu<0xB1, 0xf>(a2);
    a2 = umax(umin(a1, b1), umax(a2, b2)); a1 = umax(a1, b1);
    b1 = dppu<0x4E, 0xf>(a1); b2 = dppu<0x4E, 0xf>(a2);
    a2 = umax(umin(a1, b1), umax(a2, b2)); a1 = umax(a1, b1);
    const v4u o = lane < 4 ? (v4u){r4.x, tag, r4.y, tag} : (v4u){a1, tag, a2, tag};
    st16<SC1>(o, rws, lane < 4 ? WS_REC + L * 128 + 16 + lane * 16 : lane == 4 ? WS_REC + L * 128 : OOB);
  }
  STAMP(3);
  u32 k1 = 0, k2 = 0;
  {
    u32 go[4];
#pragma unroll
    for (int i = 0; i < 4; ++i) go[i] = WS_REC + (i * 64 + lane) * 128;
    build_copies();
    hold(t0, T_REC);
    SPIN(SPIN_G) {
      MEMFENCE();
      v4u gr[4];
#pragma unroll
      for (int i = 0; i < 4; ++i) gr[i] = ld16<SC1>(rws, go[i]);
      u32 pend = OOB;
#pragma unroll
      for (int i = 0; i < 4; ++i) {
        const bool ok = (gr[i].y == tag) & (gr[i].w == tag) & (go[i] != OOB);
        const u32 ka = ok ? gr[i].x : 0u, kb = ok ? gr[i].z : 0u;
        k2 = umax(med3u(k1, k2, ka), kb); k1 = umax(k1, ka);
        go[i] = ok ? OOB : go[i];
        pend = umin(pend, go[i]);
      }
      if (wave_all(pend == OOB)) break;
      __builtin_amdgcn_s_sleep(SLP_REC);
    }
  }
  STAMP(4);
  const u32 t1 = wave_max(k1);
  const u32 t2 = wave_max(k1 == t1 ? k2 : k1);
  const u32 idx0 = 2047u - (t1 & 0x7FFu), idx1 = 2047u - (t2 & 0x7FFu);
  const u32 myrow = (lane >> 5) ? idx1 : idx0;
  const u32 so = WS_REC + (myrow >> 3) * 128 + 16 + (myrow & 7) * 8;
  const v4u xs = ld16<0>(rq, myrow * 2048 + (j & 3) * 512 + (lane & 31) * 16);
  v2u sg = ld8<SC1>(rws, so);
  v4u stt[16];
#pragma unroll
  for (int i = 0; i < 16; ++i) stt[i] = ld16<SC1>(rws, WS_REC + (i * 16 + (lane >> 2)) * 128 + 16 + (lane & 3) * 16);
  st4<SC1>(tag, rws, (L == 0 && lane == 0) ? WS_EPOCH : OOB);
  SCHED();
  if (!wave_all(sg.y == tag)) {
    SPIN(SPIN_G) {
      MEMFENCE();
      sg = ld8<SC1>(rws, so);
      if (wave_all(sg.y == tag)) break;
    }
  }
  {
    const float mu = lo2f(sg.x), rs = __builtin_amdgcn_rcpf(hi2f(sg.x));
    v4u xn;
#pragma unroll
    for (int i = 0; i < 4; ++i) { const u32 xx = xs[i]; xn[i] = pk2((lo2f(xx) - mu) * rs, (hi2f(xx) - mu) * rs); }
    lds_st<v4u>(L_XS + lane * 16, xn);
    MEMFENCE();
    lds_stv<v4u>(C_SEL, (v4u){1u, t1, t2, 0u});
    MEMFENCE();
  }
  STAMP(5);
  SPIN(SPIN_G) {
    bool ok = true;
#pragma unroll
    for (int i = 0; i < 16; ++i) ok = ok & (stt[i].y == tag) & (stt[i].w == tag);
    if (wave_all(ok)) break;
    MEMFENCE();
#pragma unroll
    for (int i = 0; i < 16; ++i) stt[i] = ld16<SC1>(rws, WS_REC + (i * 16 + (lane >> 2)) * 128 + 16 + (lane & 3) * 16);
  }
  lds_wait_min4(C_PUB, 0);
#pragma unroll
  for (int i = 0; i < 16; ++i) {
    const float r0 = __builtin_amdgcn_rcpf(hi2f(stt[i].x)), r1 = __builtin_amdgcn_rcpf(hi2f(stt[i].z));
    lds_st<f32x2>(L_RS + i * 512 + lane * 8, (f32x2){r0, r1});
    lds_st<f32x2>(L_NB + i * 512 + lane * 8, (f32x2){-lo2f(stt[i].x) * r0, -lo2f(stt[i].z) * r1});
  }
  MEMFENCE();
  lds_stv<u32>(C_STA, 1u);
  MEMFENCE();
  STAMP(6);
  {
    const rsrc_t ro = mkrs(p.out, S * D * 2);
    u32 ra = L_OS + ((lane >> 2) & 3) * OS_CH + ((lane >> 4) * 16 + (lane & 3) * 4) * 2;
    u32 row = lane;
#pragma unroll 1
    for (u32 c = 0; c < 8; ++c) {
      lds_wait_min4(C_OUT, c);
      v2u o[4];
#pragma unroll
      for (int i = 0; i < 4; ++i) o[i] = __builtin_bit_cast(v2u, __builtin_amdgcn_ds_read_tr16_b64_v4i16((LAS s16x4*)(size_t)(ra + i * 128)));
#pragma unroll
      for (int i = 0; i < 4; ++i) {
        const u32 r = row + i * 64;
        st8<OAUX>(o[i], ro, ((r == idx0) | (r == idx1)) ? OOB : r * 2048 + L * 8);
        if (SLP_ST) { SCHED(); __builtin_amdgcn_s_sleep(SLP_ST); SCHED(); }
      }
      ra += 512; row += 256;
    }
  }
  STAMP(7);
  STAMP_FLUSH();
}

__global__ void tiny(u32* w) { if (w == nullptr) __builtin_trap(); }
extern "C" void kernel_launch(void* const* d_in, const int* in_sizes, int n_in, void* d_out, int out_size, void* d_ws,
                              size_t ws_size, hipStream_t stream) {
  Params p;
  p.q = (const u16*)d_in[0]; p.k = (const u16*)d_in[1]; p.v = (const u16*)d_in[2];
  p.shk = (const u16*)d_in[3]; p.ssmk = (const u16*)d_in[4]; p.ssmD = (const u16*)d_in[5];
  p.wr = (const u16*)d_in[7]; p.wg = (const u16*)d_in[8]; p.wu = (const u16*)d_in[9]; p.wd = (const u16*)d_in[10];
  p.out = (u16*)d_out; p.ws = (char*)d_ws;
  (void)hipFuncSetAttribute((const void*)mega, hipFuncAttributeMaxDynamicSharedMemorySize, L_END);
  hipLaunchKernelGGL(mega, dim3(256), dim3(512), L_END, stream, p);
}
```

```cpp
#define T_REC 600
#define T_KV0 600
#define T_KVS 0
#define RW 0
#define RI 99
#define RB -1
#define RFIX 2500
#include <hip/hip_runtime.h>
#include <stdint.h>
#include <utility>

typedef unsigned short u16;
typedef unsigned u32;
typedef unsigned long long u64;
typedef u32 v4u __attribute__((ext_vector_type(4)));
typedef u32 v2u __attribute__((ext_vector_type(2)));
typedef float f32x4 __attribute__((ext_vector_type(4)));
typedef float f32x2 __attribute__((ext_vector_type(2)));
typedef __bf16 bf16x8 __attribute__((ext_vector_type(8)));
typedef __bf16 bf16x2 __attribute__((ext_vector_type(2)));
typedef short s16x4 __attribute__((ext_vector_type(4)));
#define LAS __attribute__((address_space(3)))
#define CAS __attribute__((address_space(4)))
typedef __amdgpu_buffer_rsrc_t rsrc_t;

#define POL_IN 0
#define POL_Q 0
#define POL_W 0
#define WIN 10
#define DLY 3
#define NRES 4
#define EARLY_W 0
#define PD 4
#define CODE_KB 29
#define SC1 16
#define OAUX 0
#define KXAUX 16
#define NO_W 0
#define KXL 1

constexpr int S = 2048, D = 1024, F = 3584;
constexpr u32 SENT = 0xFFFFFFFFu;
constexpr u32 OOB = 0x80000000u;
constexpr u32 SPIN_G = 1u << 16;
constexpr u32 SPIN_L = 1u << 22;

constexpr u32 WS_EPOCH = 0;
constexpr u32 WS_REC = 4096;
constexpr u32 WS_KVF = WS_REC + 32768;
constexpr u32 WS_GU = WS_KVF + 131072;
constexpr u32 WS_I2B = 1u << 20;
constexpr u32 WS_KX = 4u << 20;
constexpr u32 WS_DBG = 12u << 20;
constexpr u32 WS_END = 13u << 20;

constexpr u32 HC_COPY = 4160, HC_CH = 4 * HC_COPY;
constexpr u32 L_HC = 0;
constexpr u32 UB_CH = 512 + 4096 + 64 + 4096 + 64;
constexpr u32 L_UB = L_HC + 4 * HC_CH;
constexpr u32 OS_CH = 4224;
constexpr u32 L_OS = L_UB + 4 * UB_CH;
constexpr u32 L_RS = L_OS + 4 * OS_CH;
constexpr u32 L_NB = L_RS + 8192;
constexpr u32 L_XS = L_NB + 8192;
constexpr u32 L_HA = L_XS + 1024;
constexpr u32 L_LN = L_HA + 512;
constexpr u32 L_CTL = L_LN + 64;
constexpr u32 C_OUT = L_CTL;
constexpr u32 C_SEL = L_CTL + 16;
constexpr u32 C_LN = L_CTL + 32;
constexpr u32 C_PUB = L_CTL + 48;
constexpr u32 C_STG = L_CTL + 64;
constexpr u32 C_STA = L_CTL + 68;
constexpr u32 C_TAG = L_CTL + 72;
constexpr u32 C_ZERO = L_CTL + 80;
constexpr u32 C_DN = L_CTL + 96;
constexpr u32 C_HC = L_CTL + 112;
constexpr u32 TS_W = 8320;
constexpr u32 L_END = L_CTL + 128;

struct Params {
  const u16 *q, *k, *v, *shk, *ssmk, *ssmD, *wr, *wg, *wu, *wd;
  u16* out;
  char* ws;
};

__device__ __forceinline__ rsrc_t mkrs(const void* p, u32 bytes) {
  return __builtin_amdgcn_make_buffer_rsrc((void*)p, 0, bytes, 0x00020000);
}
template <int AUX> __device__ __forceinline__ v4u ld16(rsrc_t r, u32 off) { return __builtin_amdgcn_raw_buffer_load_b128(r, off, 0, AUX); }
template <int AUX> __device__ __forceinline__ v2u ld8(rsrc_t r, u32 off) { return __builtin_amdgcn_raw_buffer_load_b64(r, off, 0, AUX); }
template <int AUX> __device__ __forceinline__ u32 ld4(rsrc_t r, u32 off) { return __builtin_amdgcn_raw_buffer_load_b32(r, off, 0, AUX); }
template <int AUX> __device__ __forceinline__ void st16(v4u v, rsrc_t r, u32 off) { __builtin_amdgcn_raw_buffer_store_b128(v, r, off, 0, AUX); }
template <int AUX> __device__ __forceinline__ void st8(v2u v, rsrc_t r, u32 off) { __builtin_amdgcn_raw_buffer_store_b64(v, r, off, 0, AUX); }
template <int AUX> __device__ __forceinline__ void st4(u32 v, rsrc_t r, u32 off) { __builtin_amdgcn_raw_buffer_store_b32(v, r, off, 0, AUX); }

__device__ __forceinline__ float asf(u32 x) { return __builtin_bit_cast(float, x); }
__device__ __forceinline__ u32 asu(float x) { return __builtin_bit_cast(u32, x); }
__device__ __forceinline__ float lo2f(u32 x) { return asf(x << 16); }
__device__ __forceinline__ float hi2f(u32 x) { return asf(x & 0xFFFF0000u); }
__device__ __forceinline__ u32 pk2(float lo, float hi) {
  f32x2 f = {lo, hi};
  return __builtin_bit_cast(u32, __builtin_convertvector(f, bf16x2));
}
__device__ __forceinline__ float rbf(float f) { return lo2f(pk2(f, f)); }
__device__ __forceinline__ float dot2(u32 a, u32 b, float c) {
  return __builtin_amdgcn_fdot2_f32_bf16(__builtin_bit_cast(bf16x2, a), __builtin_bit_cast(bf16x2, b), c, false);
}
__device__ __forceinline__ f32x4 mfma(v4u a, v4u b, f32x4 c) {
  return __builtin_amdgcn_mfma_f32_16x16x32_bf16(__builtin_bit_cast(bf16x8, a), __builtin_bit_cast(bf16x8, b), c, 0, 0, 0);
}
#define SCHED() __builtin_amdgcn_sched_barrier(0)
#define VMCNT(n) do { SCHED(); asm volatile("s_waitcnt vmcnt(%0)" ::"n"(n) : "memory"); \
    __builtin_amdgcn_s_waitcnt(((n) & 15) | (7 << 4) | (15 << 8) | (((n) >> 4) << 14)); SCHED(); } while (0)
#define LGKM0() asm volatile("s_waitcnt lgkmcnt(0)" ::: "memory")
#define MEMFENCE() asm volatile("" ::: "memory")
#define SPIN(cap) _Pragma("unroll 1") for (u32 spins = 0; spins < (cap); ++spins)

template <int CTRL, int RM> __device__ __forceinline__ float dppf(float x) {
  return asf(__builtin_amdgcn_update_dpp(0u, asu(x), CTRL, RM, 0xf, false));
}
template <int CTRL, int RM> __device__ __forceinline__ u32 dppu(u32 x) { return __builtin_amdgcn_update_dpp(0u, x, CTRL, RM, 0xf, false); }
__device__ __forceinline__ float wave_sum63(float x) {
  x += dppf<0x111, 0xf>(x); x += dppf<0x112, 0xf>(x); x += dppf<0x114, 0xf>(x); x += dppf<0x118, 0xf>(x);
  x += dppf<0x142, 0xa>(x); x += dppf<0x143, 0xc>(x);
  return x;
}
__device__ __forceinline__ u32 umax(u32 a, u32 b) { return a > b ? a : b; }
__device__ __forceinline__ u32 umin(u32 a, u32 b) { return a < b ? a : b; }
__device__ __forceinline__ u32 wave_max(u32 x) {
  x = umax(x, dppu<0x111, 0xf>(x)); x = umax(x, dppu<0x112, 0xf>(x)); x = umax(x, dppu<0x114, 0xf>(x)); x = umax(x, dppu<0x118, 0xf>(x));
  x = umax(x, dppu<0x142, 0xa>(x)); x = umax(x, dppu<0x143, 0xc>(x));
  return __builtin_amdgcn_readlane(x, 63);
}
__device__ __forceinline__ u32 med3u(u32 a, u32 b, u32 c) {
  u32 r; asm("v_med3_u32 %0, %1, %2, %3" : "=v"(r) : "v"(a), "v"(b), "v"(c)); return r;
}
__device__ __forceinline__ bool wave_all(bool p) { return __builtin_amdgcn_ballot_w64(p) == ~0ull; }
__device__ __forceinline__ u32 nosent(float f) { return umin(asu(f), SENT - 1u); }
__device__ __forceinline__ u32 rfl(u32 x) { return __builtin_amdgcn_readfirstlane(x); }

template <typename T> __device__ __forceinline__ T lds_ld(u32 a) { return *(LAS T*)(size_t)a; }
template <typename T> __device__ __forceinline__ T lds_ldv(u32 a) { return *(volatile LAS T*)(size_t)a; }
template <typename T> __device__ __forceinline__ void lds_st(u32 a, T v) { *(LAS T*)(size_t)a = v; }
template <typename T> __device__ __forceinline__ void lds_stv(u32 a, T v) { *(volatile LAS T*)(size_t)a = v; }
__device__ __forceinline__ void lds_wait_nz(u32 a) {
  SPIN(SPIN_L) {
    if (rfl(lds_ldv<u32>(a))) break;
    __builtin_amdgcn_s_sleep(1);
  }
  MEMFENCE();
}
__device__ __forceinline__ void lds_wait_min4(u32 a, u32 gt) {
  SPIN(SPIN_L) {
    const v4u f = lds_ldv<v4u>(a);
    if (rfl(umin(umin(f.x, f.y), umin(f.z, f.w))) > gt) break;
    __builtin_amdgcn_s_sleep(1);
  }
  MEMFENCE();
}

#define STAMP(i) do { if ((i) == RI) { SCHED(); ro_t = now() - t0; SCHED(); } } while (0)
#define STAMP_DECL u32 ro_t = 0
#define GAPRD()
#define STAMP_FLUSH() do { if (wave == RW && (RB < 0 || (int)blockIdx.x == RB)) { GAPRD(); SPIN(1u << 20) { if (now() - t0 >= RFIX + 5 * ro_t) break; __builtin_amdgcn_s_sleep(1); } } } while (0)

__device__ __forceinline__ void perm8(v4u* w) {
  v4u o[8];
#pragma unroll
  for (int c = 0; c < 8; ++c)
#pragma unroll
    for (int i = 0; i < 4; ++i)
      o[c][i] = __builtin_amdgcn_perm(w[2 * i + 1][c >> 1], w[2 * i][c >> 1], (c & 1) ? 0x07060302u : 0x05040100u);
#pragma unroll
  for (int c = 0; c < 8; ++c) w[c] = o[c];
}

__device__ __forceinline__ v4u rdA(u32 aA, int m) {
  v2u lo = lds_ldv<v2u>(aA + 64 * m), hi = lds_ldv<v2u>(aA + 64 * m + 8);
  return (v4u){lo.x, lo.y, hi.x, hi.y};
}

constexpr int NSTEP = 160, RING = PD + 2;
constexpr int st_pp(int s) { return s < 16 ? 0 : s < 48 ? 1 : s < 96 ? 2 : 3; }
constexpr int st_b0(int pp) { return pp == 0 ? 0 : pp == 1 ? 16 : pp == 2 ? 48 : 96; }
constexpr int st_ls(int s) { return s - st_b0(st_pp(s)); }
constexpr bool st_diag(int s) { return st_ls(s) >= 16 * st_pp(s); }
constexpr int st_m0(int s) { return st_diag(s) ? (st_ls(s) - 16 * st_pp(s)) / 2 : st_ls(s) / (2 * st_pp(s)); }
constexpr int st_bb(int s) { return st_diag(s) ? 2 * st_pp(s) + (st_ls(s) & 1) : st_ls(s) % (2 * st_pp(s)); }
constexpr bool st_needP(int s) { return s < NSTEP && st_bb(s) == 0 && 2 * st_pp(s) + 1 >= NRES; }
constexpr bool st_needA(int s) { return s < NSTEP && 2 * st_pp(s) - st_bb(s) >= NRES; }
constexpr bool st_light(int s) { return !st_needA(s + PD) && !st_needP(s + PD + 1); }
constexpr int FIN0 = 4;
constexpr int st_piece(int s) {
  if (st_pp(s) == 0 || st_ls(s) < FIN0 || !st_light(s)) return -1;
  int n = 0;
  for (int t = st_b0(st_pp(s)) + FIN0; t < s; ++t) n += st_light(t) ? 1 : 0;
  return n < 10 ? n : -1;
}

__device__ __forceinline__ void mfma_acc(f32x4& c, v4u a, v4u b) {
  asm volatile("v_mfma_f32_16x16x32_bf16 %0, %1, %2, %0" : "+v"(c) : "v"(a), "v"(b));
}
__device__ __forceinline__ void mfma_one(f32x4& c, v4u a, v4u b) {
  asm volatile("v_mfma_f32_16x16x32_bf16 %0, %1, %2, 1.0" : "=&v"(c) : "v"(a), "v"(b));
}

struct Conv {
  v4u A[8 * NRES], AD, Bq[RING];
  v2u Al[RING], Ah[RING], Pl[2], Ph[2];
  f32x4 acc[2][2], rs[2], nb[2];
  float o[4];
  v2u xx[2];
  u32 aA, aB, aX, aS, osw, flg, pk;

  template <int s> __device__ __forceinline__ void rdB() {
    if constexpr (s < NSTEP) Bq[s % RING] = lds_ldv<v4u>(aB + (8 * st_bb(s) - st_m0(s) + 7) * 64);
  }
  template <int s, int h> __device__ __forceinline__ void rdAq() {
    if constexpr (st_needA(s)) {
      constexpr int m = st_m0(s) + 8 * (2 * st_pp(s) - st_bb(s));
      if constexpr (h == 0) Al[s % RING] = lds_ldv<v2u>(aA + 64 * m); else Ah[s % RING] = lds_ldv<v2u>(aA + 64 * m + 8);
    }
  }
  template <int s, int h> __device__ __forceinline__ void rdP() {
    if constexpr (st_needP(s)) {
      constexpr int m = st_m0(s) + 8 * (2 * st_pp(s) + 1);
      if constexpr (h == 0) Pl[st_m0(s) & 1] = lds_ldv<v2u>(aA + 64 * m); else Ph[st_m0(s) & 1] = lds_ldv<v2u>(aA + 64 * m + 8);
    }
  }
  template <int s> __device__ __forceinline__ v4u ringA() {
    const v2u l = Al[s % RING], h = Ah[s % RING];
    return (v4u){l.x, l.y, h.x, h.y};
  }
  template <int pp> __device__ __forceinline__ void fin_load() {
#pragma unroll
    for (int e = 0; e < 2; ++e) {
      xx[e] = lds_ldv<v2u>(aX + (2 * pp + e) * 512);
      rs[e] = lds_ldv<f32x4>(aS + (2 * pp + e) * 1024);
      nb[e] = lds_ldv<f32x4>(aS + (2 * pp + e) * 1024 + 8192);
    }
  }
  template <int pp, int pc> __device__ __forceinline__ void piece() {
    constexpr int e = pc / 5, r = pc % 5;
    if constexpr (r < 4) {
      const u32 xw = xx[e][r >> 1];
      const float x = (r & 1) ? hi2f(xw) : lo2f(xw);
      o[r] = __builtin_fmaf(__builtin_fmaf(x, rs[e][r], nb[e][r]), acc[pp & 1][e][r], x);
      asm volatile("" : "+v"(o[r]));
    } else {
      lds_stv<v2u>(osw + (2 * pp + e) * 512, (v2u){pk2(o[0], o[1]), pk2(o[2], o[3])});
      lds_stv<u32>(flg, 2 * pp + e + 1);
    }
  }
  template <int s> __device__ __forceinline__ void step() {
    constexpr int pp = st_pp(s), m0 = st_m0(s), bb = st_bb(s), c0 = 2 * pp, c1 = c0 + 1, ls = st_ls(s);
    if constexpr (pp > 0 && ls == 16 * pp - PD - 3) pk = lds_ldv<u32>(C_STG);
    if constexpr (pp > 0 && ls == 16 * pp - PD) {
      if (rfl(pk) <= (u32)pp) {
        SPIN(SPIN_L) { if (rfl(lds_ldv<u32>(C_STG)) > (u32)pp) break; __builtin_amdgcn_s_sleep(1); }
      }
    }
    if constexpr (s == 13) pk = lds_ldv<u32>(C_STA);
    if constexpr (s == 16) { if (rfl(pk) == 0) lds_wait_nz(C_STA); }
    const v4u Bf = Bq[s % RING];
    v4u A1, A0;
    if constexpr (c1 - bb < NRES) A1 = A[m0 + 8 * (c1 - bb)];
    else if constexpr (bb == 0) A1 = (v4u){Pl[m0 & 1].x, Pl[m0 & 1].y, Ph[m0 & 1].x, Ph[m0 & 1].y};
    else A1 = ringA<s - 1>();
    if constexpr (bb <= c0) {
      if constexpr (c0 - bb < NRES) A0 = A[m0 + 8 * (c0 - bb)]; else A0 = ringA<s>();
    }
    if constexpr (st_needA(s)) asm volatile("" :: "v"(Ah[s % RING]));
    if constexpr (ls == 0) mfma_one(acc[pp & 1][1], A1, Bf); else mfma_acc(acc[pp & 1][1], A1, Bf);
    rdB<s + PD>();
    rdAq<s + PD, 0>();
    rdP<s + PD + 1, 0>();
    SCHED();
    if constexpr (bb <= c0) {
      if constexpr (ls == 0) mfma_one(acc[pp & 1][0], A0, Bf); else mfma_acc(acc[pp & 1][0], A0, Bf);
    }
    rdAq<s + PD, 1>();
    rdP<s + PD + 1, 1>();
    if constexpr (st_piece(s) >= 0) piece<pp - 1, st_piece(s)>();
    if constexpr (m0 == 0 && bb == c1) mfma_acc(acc[pp & 1][1], AD, Bf);
    if constexpr (m0 == 0 && bb == c0) mfma_acc(acc[pp & 1][0], AD, Bf);
    if constexpr (pp > 0 && ls == 0) fin_load<pp - 1>();
    if constexpr (s == NSTEP - 4) fin_load<3>();
    SCHED();
  }
  template <int... Is> __device__ __forceinline__ void pre(std::integer_sequence<int, Is...>) { (rdB<Is>(), ...); }
  template <int... Is> __device__ __forceinline__ void run(std::integer_sequence<int, Is...>) { (step<Is>(), ...); }
  template <int... Is> __device__ __forceinline__ void last(std::integer_sequence<int, Is...>) { (piece<3, Is>(), ...); }
  __device__ __forceinline__ void stream() {
    pre(std::make_integer_sequence<int, PD>{});
    run(std::make_integer_sequence<int, NSTEP>{});
    asm volatile("s_nop 7" : "+v"(acc[1][0]), "+v"(acc[1][1]));
    last(std::make_integer_sequence<int, 10>{});
  }
  __device__ __forceinline__ void setup(int w, int lane, u32 Dbits) {
    const int M = lane & 15, q4 = lane >> 4;
    const u32 ub = L_UB + w * UB_CH, hb = L_HC + w * HC_CH;
    aA = hb + ((M + 1) & 3) * HC_COPY + ((M + 1) >> 2) * 8 + q4 * 16;
#pragma unroll
    for (int m = 0; m < 8 * NRES; ++m) A[m] = rdA(aA, m);
    const int jd = 15 - M - 8 * q4;
#pragma unroll
    for (int i = 0; i < 4; ++i) AD[i] = jd == 2 * i ? Dbits : jd == 2 * i + 1 ? (Dbits << 16) : 0u;
    aB = ub + 512 - 7 * 64 + (2 * M + 1 - q4) * 16;
    aX = ub + 512 + 4096 + 64 + (16 * M + 4 * q4) * 2;
    aS = L_RS + (16 * M + 4 * q4) * 4;
    osw = L_OS + w * OS_CH + (16 * M + 4 * q4) * 2;
    flg = C_OUT + w * 4;
    asm volatile("" : "+v"(aB), "+v"(aA), "+v"(aX), "+v"(aS), "+v"(flg), "+v"(osw), "+v"(AD));
#pragma unroll
    for (int m = 0; m < 8 * NRES; ++m) asm volatile("" : "+v"(A[m]));
  }
};

#define T_W 0
#define SLP_ST 2
#define SLP_REC 4
#define SLP_KV 2

__device__ __forceinline__ u32 get_tag() {
  u32 tag = 0;
  SPIN(SPIN_L) {
    const v2u f = lds_ldv<v2u>(C_TAG);
    tag = rfl(f.y);
    if (rfl(f.x)) break;
    __builtin_amdgcn_s_sleep(1);
  }
  return tag;
}

struct Lander {
  v4u in[4][8];
  u32 aI, landed;
  template <int s> __device__ __forceinline__ void pending() {
#pragma unroll
    for (int t = 0; t < s; ++t)
      if (landed == (u32)t) {
#pragma unroll
        for (int i = 0; i < 8; ++i) lds_st<v4u>(aI + t * 1024 + i * 128, in[t][i]);
        MEMFENCE();
        lds_stv<u32>(C_STG, t + 1);
        MEMFENCE();
        landed = t + 1;
      }
  }
};

__device__ __forceinline__ u32 now() { return (u32)__builtin_amdgcn_s_memrealtime(); }
__device__ __forceinline__ void hold(u32 t0, u32 dt) {
  SPIN(4096) { if (now() - t0 >= dt) break; __builtin_amdgcn_s_sleep(1); }
}
template <int AUX> __device__ __forceinline__ v4u ld16s(rsrc_t r, u32 off, u32 soff) { return __builtin_amdgcn_raw_buffer_load_b128(r, off, soff, AUX); }

typedef u32 v16u __attribute__((ext_vector_type(16)));
typedef u32 v8u __attribute__((ext_vector_type(8)));
#define ENDW() do { STAMP_FLUSH(); asm volatile("s_endpgm" : "=v"(T[0]), "=v"(T[1]), "=v"(T[2]), "=v"(T[3]), "=v"(T[4]), "=v"(T[5]), \
    "=v"(T[6]), "=v"(T[7]), "=v"(T[8]), "=v"(T[9]), "=v"(T[10]), "=v"(T[11]), "=v"(hn[0]), "=v"(hn[1]), "=v"(hn[2]), "=v"(hn[3]) :: "memory"); } while (0)

__global__ void __launch_bounds__(512, 2) mega(Params p_unused) {
  extern __shared__ __attribute__((aligned(16))) char smem[];
  u64 pc0;
  v16u ka;
  v8u kb;
  asm volatile("s_load_dwordx16 %1, %3, 0x0\n\ts_load_dwordx8 %2, %3, 0x40\n\ts_getpc_b64 %0"
               : "=&s"(pc0), "=&s"(ka), "=&s"(kb) : "s"(__builtin_amdgcn_kernarg_segment_ptr()));
  const int tid = threadIdx.x;
  const int wave = __builtin_amdgcn_readfirstlane(tid >> 6);
  const int lane = tid & 63;
  const int b = blockIdx.x, xcd = b & 7, rk = b >> 3;
  const u32 codepf = ld4<0>(mkrs((const void*)(pc0 & ~255ull), CODE_KB * 1024), lane < 8 ? (rk & 3) * 8192 + wave * 1024 + lane * 128 : OOB);
  SCHED();
  const u32 t0 = now();
  const int L = xcd * 32 + rk;
  STAMP_DECL;
  STAMP(0);
  lds_st<v4u>(L_CTL + (tid & 7) * 16, (v4u){0, 0, 0, 0});
  const int jw = wave & 3, cg = L >> 4, rb = L & 15, ch = 4 * L + jw;
  const bool isconv = wave < 4;
  const u32 toff = isconv ? (u32)((jw * 512 + rb * 32 + (lane >> 3) * 4) * 2048 + cg * 128 + (lane & 7) * 16) : (u32)(lane * 16);
  const bool ffn = rk < 28;
  const int j = xcd * 28 + rk;
  u32 c0off = ((wave >> 1) == 2 && ffn) ? WS_GU + j * 1024 + jw * 512 + lane * 16 + (lane >> 5) * OOB
              : wave == 6 ? WS_I2B + L * 8192 + (lane >> 1) * 128 + (lane & 1) * 16 : OOB;
  u32 c1off = (wave == 6 && lane < 48) ? c0off + 4096 : OOB;
  asm volatile("" : "+v"(c0off), "+v"(c1off));
  asm volatile("s_waitcnt lgkmcnt(0)" : "+s"(ka), "+s"(kb) :: "memory");
  __builtin_amdgcn_s_barrier();
  SCHED();
  Params p;
#define KP(v, i) (((u64)v[2 * (i) + 1] << 32) | v[2 * (i)])
  p.q = (const u16*)KP(ka, 0); p.k = (const u16*)KP(ka, 1); p.v = (const u16*)KP(ka, 2); p.shk = (const u16*)KP(ka, 3);
  p.ssmk = (const u16*)KP(ka, 4); p.ssmD = (const u16*)KP(ka, 5); p.wr = (const u16*)KP(ka, 6); p.wg = (const u16*)KP(ka, 7);
  p.wu = (const u16*)KP(kb, 0); p.wd = (const u16*)KP(kb, 1); p.out = (u16*)KP(kb, 2); p.ws = (char*)KP(kb, 3);
#undef KP
  const rsrc_t rws = mkrs(p.ws, WS_END);
  v2u hn[4];
  v4u T[12];
  {
    const rsrc_t r1 = mkrs(isconv ? p.k : p.q + (8 * L + 2 * jw) * D, S * D * 2);
    const rsrc_t r2 = mkrs(isconv ? p.v : p.wr, isconv ? S * D * 2 : D * 2);
    const rsrc_t r3 = mkrs(isconv ? p.q : p.ssmk + ch * S, S * D * 2);
    const u32 str = isconv ? 2048 : 1024;
    for (int i = 0; i < (isconv ? jw : 0); ++i) __builtin_amdgcn_s_sleep(DLY);
    SCHED();
#pragma unroll
    for (int i = 0; i < 4; ++i) T[i] = ld16s<POL_IN>(r1, toff, i * str);
#pragma unroll
    for (int i = 0; i < 4; ++i) T[4 + i] = ld16s<POL_IN>(r2, toff, i * str);
    SCHED();
    st16<SC1>((v4u){SENT, SENT, SENT, SENT}, rws, c0off);
    st16<SC1>((v4u){SENT, SENT, SENT, SENT}, rws, c1off);
    SCHED();
#pragma unroll
    for (int i = 0; i < 4; ++i) T[8 + i] = ld16s<POL_IN>(r3, toff, i * str);
    SCHED();
  }

  if (isconv) {
    v4u R[19];
    const int w = jw;
    const int g = lane & 7, rr = lane >> 3;
    const rsrc_t rk_ = mkrs(p.k, S * D * 2), rs = mkrs(p.shk, D * 8);
#pragma unroll
    for (int i = 0; i < 3; ++i) R[i] = ld16<0>(rk_, toff - (3 - i) * 2048);
#pragma unroll
    for (int i = 0; i < 4; ++i) R[11 + i] = ld16<0>(rs, (cg * 64 + g * 8) * 8 + i * 16);
    const u32 Dw = *(const CAS u32*)(p.ssmD + (ch & ~1));
    SCHED();
#pragma unroll
    for (int i = 0; i < 4; ++i) { R[3 + i] = T[i]; R[7 + i] = T[4 + i]; R[15 + i] = T[8 + i]; }
    const u32 hb = L_OS + w * TS_W;
    if (lane < 32) lds_st<v4u>(L_UB + w * UB_CH + lane * 16, (v4u){0, 0, 0, 0});
    STAMP(1);
    VMCNT(0);
    asm volatile("" :: "v"(codepf));
    STAMP(2);
    const u32 tw = hb + g * 1040 + (rr >> 1) * 16;
#pragma unroll
    for (int c = 0; c < 8; ++c) {
      const u32 wa = R[11 + (c >> 1)][(c & 1) * 2], wb = R[11 + (c >> 1)][(c & 1) * 2 + 1];
      const u32 sel = (c & 1) ? 0x07060302u : 0x05040100u;
      u32 P[6];
      float kv[4];
#pragma unroll
      for (int i = 0; i < 6; ++i) P[i] = __builtin_amdgcn_perm(R[i][c >> 1], R[i + 1][c >> 1], sel);
#pragma unroll
      for (int i = 0; i < 4; ++i) {
        const u32 vx = R[7 + i][c >> 1];
        kv[i] = dot2(P[i + 2], wa, dot2(P[i], wb, 0.f)) * ((c & 1) ? hi2f(vx) : lo2f(vx));
      }
      lds_st<v2u>(tw + c * 128 + ((rr & 1) ? 0 : 8), (v2u){pk2(kv[3], kv[2]), pk2(kv[1], kv[0])});
    }
    SCHED();
#pragma unroll
    for (int c = 0; c < 8; ++c) {
      const u32 sel = (c & 1) ? 0x07060302u : 0x05040100u;
      lds_st<v2u>(tw + c * 128 + 64 + ((rr & 1) ? 8 : 0),
                  (v2u){__builtin_amdgcn_perm(R[16][c >> 1], R[15][c >> 1], sel), __builtin_amdgcn_perm(R[18][c >> 1], R[17][c >> 1], sel)});
    }
    MEMFENCE();
#pragma unroll
    for (int i = 0; i < 8; ++i) {
      v4u t = lds_ld<v4u>(hb + i * 1040 + lane * 16);
      st16<KXAUX>(t, rws, WS_KX + (cg * 64 + w * 16 + rb) * 8192 + i * 1024 + lane * 16);
    }
    STAMP(3);
    const u32 tag = get_tag();
    VMCNT(0);
    st4<SC1>(tag, rws, lane == 0 ? WS_KVF + (cg * 64 + w * 16 + rb) * 128 : OOB);
    lds_stv<u32>(C_PUB + w * 4, 1u);
    STAMP(4);
    SCHED();
    lds_wait_nz(C_HC + w * 4);
    Conv cv;
    cv.setup(w, lane, (ch & 1) ? (Dw >> 16) : (Dw & 0xFFFFu));
    STAMP(5);
    lds_wait_nz(C_STG);
    STAMP(6);
    cv.stream();
    STAMP(7);
    ENDW();
  }

  const rsrc_t rq = mkrs(p.q, S * D * 2);
  v4u R[48];
  u32 epoch = 0;
  {
    if (jw == 3) epoch = *(const CAS u32*)(p.ws + WS_EPOCH);
    const rsrc_t rh = mkrs(p.ssmk, S * D * 2);
#pragma unroll
    for (int i = 0; i < 4; ++i) hn[i] = ld8<0>(rh, ch * 4096 + (i * 64 + lane) * 16 + 16);
    SCHED();
  }
  const rsrc_t rw = mkrs(jw == 0 ? p.wg : p.wu, (ffn && jw < 2 && !NO_W) ? D * F * 2 : 0);
  const u32 wo = ((j & 3) * 256 + (lane >> 3) * 8) * (F * 2) + (j >> 2) * 128 + (lane & 7) * 16;
#pragma unroll
  for (int i = 0; i < EARLY_W; ++i) R[i] = ld16<POL_W>(rw, wo + ((i >> 3) * 64 + (i & 7)) * (F * 2));
  SCHED();
  asm volatile("" : "+s"(epoch));
  if (jw == 3) lds_stv<v2u>(C_TAG, (v2u){1u, epoch + 1u});
  STAMP(1);
  {
    float sa = 0.f, sb = 0.f, qa = 0.f, qb = 0.f, ra = 0.f, rb_ = 0.f, sw = 0.f;
#pragma unroll
    for (int i = 0; i < 8; ++i) {
      const u32 a = T[i >> 2][i & 3], bq = T[2 + (i >> 2)][i & 3], wv = T[4 + (i >> 2)][i & 3];
      sa = dot2(a, 0x3f803f80u, sa); sb = dot2(bq, 0x3f803f80u, sb);
      qa = dot2(a, a, qa); qb = dot2(bq, bq, qb);
      ra = dot2(a, wv, ra); rb_ = dot2(bq, wv, rb_);
      sw = dot2(wv, 0x3f803f80u, sw);
    }
    sa = wave_sum63(sa); sb = wave_sum63(sb); qa = wave_sum63(qa); qb = wave_sum63(qb);
    ra = wave_sum63(ra); rb_ = wave_sum63(rb_); sw = wave_sum63(sw);
    const float ma = sa * (1.f / D), mb = sb * (1.f / D);
    const float va = __builtin_fmaxf(__builtin_fmaf(-ma, ma, qa * (1.f / D)), 0.f), vb = __builtin_fmaxf(__builtin_fmaf(-mb, mb, qb * (1.f / D)), 0.f);
    const u32 mm = pk2(ma, mb), v1 = pk2(va, vb);
    const u32 v2 = pk2(lo2f(v1) + 1e-5f, hi2f(v1) + 1e-5f);
    const u32 sd = pk2(__builtin_amdgcn_sqrtf(lo2f(v2)), __builtin_amdgcn_sqrtf(hi2f(v2)));
    u32 rp = pk2(__builtin_fmaf(-lo2f(mm), sw, ra) * __builtin_amdgcn_rcpf(lo2f(sd)),
                 __builtin_fmaf(-hi2f(mm), sw, rb_) * __builtin_amdgcn_rcpf(hi2f(sd)));
    const u32 neg = ((rp >> 15) & 0x00010001u) * 0xFFFFu;
    rp = rp ^ (neg | 0x80008000u);
    const u32 inv = 2047u - (u32)(8 * L + 2 * jw);
    v4u rec = {(mm & 0xFFFFu) | (sd << 16), (mm >> 16) | (sd & 0xFFFF0000u), (rp << 16) | inv, (rp & 0xFFFF0000u) | (inv - 1u)};
    asm volatile("" : "+v"(rec) : "v"(codepf));
    VMCNT(8 + EARLY_W);
    if (lane == 63) {
      lds_st<v4u>(L_LN + jw * 16, rec);
      MEMFENCE();
      lds_stv<u32>(C_LN + jw * 4, 1u);
    }
    MEMFENCE();
  }
  STAMP(2);
  auto build_copies = [&]() {
    const u32 hb = L_HC + jw * HC_CH;
#pragma unroll
    for (int i = 0; i < 4; ++i) {
      const v4u d = T[8 + i];
      const u32 e0 = __builtin_amdgcn_alignbit(d.y, d.x, 16), e1 = __builtin_amdgcn_alignbit(d.z, d.y, 16),
                e2 = __builtin_amdgcn_alignbit(d.w, d.z, 16), e3 = __builtin_amdgcn_alignbit(hn[i].x, d.w, 16),
                e4 = __builtin_amdgcn_alignbit(hn[i].y, hn[i].x, 16);
      const u32 a = hb + 32 + (i * 64 + lane) * 16;
      lds_st<v4u>(a, d);
      lds_st<v4u>(a + HC_COPY, (v4u){e0, e1, e2, e3});
      lds_st<v4u>(a + 2 * HC_COPY, (v4u){d.y, d.z, d.w, hn[i].x});
      lds_st<v4u>(a + 3 * HC_COPY, (v4u){e1, e2, e3, e4});
    }
    {
      const u32 s0 = rfl(T[8].x), s1 = rfl(T[8].y);
      const int cb = lane & 3;
      const u32 w3 = cb == 0 ? 0u : cb == 1 ? (s0 << 16) : cb == 2 ? s0 : ((s0 >> 16) | (s1 << 16));
      const v4u z = {0, 0, lane == 7 ? (s0 << 16) : 0u, (lane & 4) ? w3 : 0u};
      if (lane < 8) lds_st<v4u>(hb + cb * HC_COPY + (lane >> 2) * 16, z);
    }
    MEMFENCE();
    SCHED();
    lds_stv<u32>(C_HC + jw * 4, 1u);
    MEMFENCE();
    SCHED();
  };
  if (jw < 3) build_copies();

  if (jw < 2) {
    if (!ffn) ENDW();
    const int q4 = lane >> 4, N = lane & 15;
    const rsrc_t rd = mkrs(p.wd, NO_W ? 0 : D * F * 2);
    const u32 dofs = ((j >> 2) * 64 + q4 * 8) * 2048 + (j & 3) * 512 + jw * 256 + N * 16;
    if (T_W) hold(t0, T_W);
#pragma unroll
    for (int i = EARLY_W; i < 48; ++i) {
      if (i >= WIN && i < 32 + WIN) VMCNT(WIN - 1);
      if (i < 32) R[i] = ld16<POL_W>(rw, wo + ((i >> 3) * 64 + (i & 7)) * (F * 2));
      else R[i] = ld16<POL_W>(rd, dofs + (((i >> 3) & 1) * 32 + (i & 7)) * 2048);
      SCHED();
      if (i >= WIN + 8 && ((i - WIN) & 7) == 0 && i - WIN <= 24) { perm8(&R[i - WIN - 8]); SCHED(); }
    }
    static_assert(WIN <= 16 && WIN >= EARLY_W, "");
    perm8(&R[24]);
#pragma unroll
    for (int i = 0; i < 32; ++i) asm volatile("" : "+v"(R[i]));
    u32 ax = L_XS + ((lane >> 1) & 1) * 512 + q4 * 32 + (lane & 1) * 16;
    const bool kap = (lane >> 3) & 1;
    u32 oo = lane < 16 ? WS_GU + j * 1024 + jw * 512 + (lane & 7) * 64 + (kap ? 32 : 0) : OOB;
    asm volatile("" : "+v"(ax), "+v"(oo));
    STAMP(3);
    lds_wait_nz(C_SEL);
    __builtin_amdgcn_s_setprio(2);
    STAMP(4);
    {
      f32x4 acc[8];
#pragma unroll
      for (int c = 0; c < 8; ++c) acc[c] = (f32x4){0, 0, 0, 0};
#pragma unroll
      for (int bk = 0; bk < 4; ++bk) {
        const v4u X = lds_ld<v4u>(ax + bk * 128);
#pragma unroll
        for (int c = 0; c < 8; ++c) acc[c] = mfma(X, R[bk * 8 + c], acc[c]);
      }
      u32 o[8];
#pragma unroll
      for (int cc = 0; cc < 4; ++cc)
#pragma unroll
        for (int t = 0; t < 2; ++t) {
          const float keep = kap ? acc[4 + cc][2 * t + 1] : acc[cc][2 * t], send = kap ? acc[cc][2 * t + 1] : acc[4 + cc][2 * t];
          o[cc * 2 + t] = nosent(keep + dppf<0x128, 0xf>(send));
        }
      st16<SC1>((v4u){o[0], o[1], o[2], o[3]}, rws, oo);
      st16<SC1>((v4u){o[4], o[5], o[6], o[7]}, rws, oo + 16);
    }
    STAMP(5);
    SCHED();
    perm8(&R[32]);
    perm8(&R[40]);
#pragma unroll
    for (int i = 32; i < 48; ++i) asm volatile("" : "+v"(R[i]));
    const u32 ha = L_HA + jw * 256;
    u32 ah[2][2];
#pragma unroll
    for (int hf = 0; hf < 2; ++hf)
#pragma unroll
      for (int ks = 0; ks < 2; ++ks) {
        ah[hf][ks] = (N >> 1) == hf ? ha + (N & 1) * 128 + ks * 64 + q4 * 16 : C_ZERO;
        asm volatile("" : "+v"(ah[hf][ks]));
      }
    u32 o2 = N < 4 ? WS_I2B + ((j & 3) * 64 + jw * 32 + q4 * 8 + (N >> 1)) * 8192 + (j >> 2) * 128 + (N & 1) * 16 : OOB;
    u32 po = WS_GU + (j & ~3) * 1024 + lane * 8;
    asm volatile("" : "+v"(o2), "+v"(po));
    STAMP(6);
    v2u pv[8];
    SPIN(SPIN_G) {
      MEMFENCE();
      u32 mx = 0;
#pragma unroll
      for (int i = 0; i < 8; ++i) pv[i] = ld8<SC1>(rws, po + i * 512);
#pragma unroll
      for (int i = 0; i < 8; ++i) mx = umax(mx, umax(pv[i].x, pv[i].y));
      if (wave_all(mx != SENT)) break;
      __builtin_amdgcn_s_sleep(2);
    }
    STAMP(7);
    const float g0 = (asf(pv[0].x) + asf(pv[2].x)) + (asf(pv[4].x) + asf(pv[6].x)), g1 = (asf(pv[0].y) + asf(pv[2].y)) + (asf(pv[4].y) + asf(pv[6].y));
    const float u0 = (asf(pv[1].x) + asf(pv[3].x)) + (asf(pv[5].x) + asf(pv[7].x)), u1 = (asf(pv[1].y) + asf(pv[3].y)) + (asf(pv[5].y) + asf(pv[7].y));
    const u32 gp = pk2(g0, g1), up = pk2(u0, u1);
    const float ga = lo2f(gp), gb = hi2f(gp);
    const u32 sp = pk2(ga * __builtin_amdgcn_rcpf(1.f + __expf(-ga)), gb * __builtin_amdgcn_rcpf(1.f + __expf(-gb)));
    const u32 hp = pk2(lo2f(sp) * lo2f(up), hi2f(sp) * hi2f(up));
    lds_st<u16>(ha + lane * 2, (u16)hp);
    lds_st<u16>(ha + 128 + lane * 2, (u16)(hp >> 16));
    MEMFENCE();
    f32x4 acc[4];
#pragma unroll
    for (int c = 0; c < 4; ++c) acc[c] = (f32x4){0, 0, 0, 0};
#pragma unroll
    for (int ks = 0; ks < 2; ++ks)
#pragma unroll
      for (int hf = 0; hf < 2; ++hf) {
        const v4u Hb = lds_ld<v4u>(ah[hf][ks]);
#pragma unroll
        for (int cc = 0; cc < 4; ++cc) acc[cc] = mfma(R[32 + ks * 8 + hf * 4 + cc], Hb, acc[cc]);
      }
#pragma unroll
    for (int r = 0; r < 4; ++r)
      st16<SC1>((v4u){nosent(acc[0][r]), nosent(acc[1][r]), nosent(acc[2][r]), nosent(acc[3][r])}, rws, o2 + r * 2 * 8192);
    lds_stv<u32>(C_DN, 1u);
    STAMP(8);
    ENDW();
  }
  if (jw == 2) {
    __builtin_amdgcn_s_setprio(2);
    {
      const u32 tag = get_tag();
      Lander ld;
      ld.aI = L_UB + ((lane >> 3) & 3) * UB_CH + 512 + ((lane & 4) ? 4096 : 0) + (lane >> 5) * 64 + (lane & 7) * 16;
      const u32 io = WS_KX + (cg * 64 + (lane >> 5)) * 8192 + (L & 15) * 512 + (lane & 31) * 16;
#define IOFF(s_, i) (((s_) * 16 + (i) * 2) * 8192)
      ld.landed = 0;
      const u32 kfo = WS_KVF + (cg * 64 + lane) * 128, pubo = C_PUB + (lane >> 4) * 4;
      bool seen = false;
      const u32 tgate = T_KV0 + (lane >> 4) * T_KVS;
#define STAGE(s_) \
      SPIN(SPIN_L) { \
        if (((__builtin_amdgcn_ballot_w64(seen) >> (16 * s_)) & 0xFFFFull) == 0xFFFFull) break; \
        const bool act = (lds_ldv<u32>(pubo) != 0) & !seen & (now() - t0 >= tgate); \
        MEMFENCE(); \
        if (__builtin_amdgcn_ballot_w64(act) == 0ull) { ld.pending<s_>(); __builtin_amdgcn_s_sleep(1); continue; } \
        const u32 f = ld4<SC1>(rws, act ? kfo : OOB); \
        ld.pending<s_>(); \
        seen = seen | (act & (f == tag)); \
        if (((__builtin_amdgcn_ballot_w64(seen) >> (16 * s_)) & 0xFFFFull) == 0xFFFFull) break; \
        __builtin_amdgcn_s_sleep(SLP_KV); \
      } \
      MEMFENCE(); \
      STAMP(3 + s_); \
      _Pragma("unroll") for (int i = 0; i < 8; ++i) ld.in[s_][i] = ld16<SC1>(rws, io + IOFF(s_, i)); \
      SCHED();
      STAGE(0) STAGE(1) STAGE(2) STAGE(3)
#undef STAGE
      ld.pending<4>();
    }
    STAMP(7);
    lds_wait_nz(C_SEL);
    const v4u ks4 = lds_ldv<v4u>(C_SEL);
    const u32 kk = (lane & 1) ? ks4.z : ks4.y;
    const u32 frow = 2047u - (kk & 0x7FFu), kx = kk >> 16;
    const float rsel = lo2f((kx & 0x8000u) ? (kx ^ 0x8000u) : (kx ^ 0xFFFFu));
    float sig = rbf(__builtin_amdgcn_rcpf(1.f + __expf(-rsel)));
    u32 pa = WS_I2B + L * 8192 + (lane >> 1) * 128 + (lane & 1) * 16;
    u32 pb_ = lane < 48 ? pa + 4096 : OOB;
    asm volatile("" : "+v"(pa), "+v"(pb_), "+v"(sig));
    if (ffn) lds_wait_nz(C_DN); else __builtin_amdgcn_s_sleep(40);
    __builtin_amdgcn_s_sleep(4);
    v4u e0, e1;
    SPIN(SPIN_G) {
      MEMFENCE();
      e0 = ld16<SC1>(rws, pa); e1 = ld16<SC1>(rws, pb_);
      const u32 mx = umax(umax(umax(e0.x, e0.y), umax(e0.z, e0.w)), umax(umax(e1.x, e1.y), umax(e1.z, e1.w)));
      if (wave_all(mx != SENT)) break;
      __builtin_amdgcn_s_sleep(2);
    }
    STAMP(8);
    float y[4];
#pragma unroll
    for (int c = 0; c < 4; ++c) {
      const u32 ua = e0[c], ub_ = e1[c];
      float s = asf(ua) + asf(ub_);
      s += dppf<0x122, 0xf>(s); s += dppf<0x124, 0xf>(s); s += dppf<0x128, 0xf>(s);
      const auto r16 = __builtin_amdgcn_permlane16_swap(asu(s), asu(s), false, false);
      s = asf(r16[0]) + asf(r16[1]);
      const auto r32 = __builtin_amdgcn_permlane32_swap(asu(s), asu(s), false, false);
      y[c] = rbf(rbf(asf(r32[0]) + asf(r32[1])) * sig);
    }
    lds_wait_min4(C_OUT, (2047u - (umin(rfl(ks4.y) & 0x7FFu, rfl(ks4.z) & 0x7FFu))) >> 8);
    float o[4];
#pragma unroll
    for (int c = 0; c < 4; ++c) o[c] = lo2f(lds_ld<u16>(L_OS + c * OS_CH + frow * 2)) + y[c];
    st8<OAUX>((v2u){pk2(o[0], o[1]), pk2(o[2], o[3])}, mkrs(p.out, S * D * 2), lane < 2 ? frow * 2048 + L * 8 : OOB);
    STAMP(9);
    ENDW();
  }

  const u32 tag = epoch + 1u;
  __builtin_amdgcn_s_setprio(2);
  lds_wait_min4(C_LN, 0);
  {
    const v4u r4 = lds_ld<v4u>(L_LN + (lane & 3) * 16);
    u32 a1 = umax(r4.z, r4.w), a2 = umin(r4.z, r4.w);
    u32 b1 = dppu<0xB1, 0xf>(a1), b2 = dppu<0xB1, 0xf>(a2);
    a2 = umax(umin(a1, b1), umax(a2, b2)); a1 = umax(a1, b1);
    b1 = dppu<0x4E, 0xf>(a1); b2 = dppu<0x4E, 0xf>(a2);
    a2 = umax(umin(a1, b1), umax(a2, b2)); a1 = umax(a1, b1);
    const v4u o = lane < 4 ? (v4u){r4.x, tag, r4.y, tag} : (v4u){a1, tag, a2, tag};
    st16<SC1>(o, rws, lane < 4 ? WS_REC + L * 128 + 16 + lane * 16 : lane == 4 ? WS_REC + L * 128 : OOB);
  }
  STAMP(3);
  u32 k1 = 0, k2 = 0;
  {
    u32 go[4];
#pragma unroll
    for (int i = 0; i < 4; ++i) go[i] = WS_REC + (i * 64 + lane) * 128;
    build_copies();
    hold(t0, T_REC);
    SPIN(SPIN_G) {
      MEMFENCE();
      v4u gr[4];
#pragma unroll
      for (int i = 0; i < 4; ++i) gr[i] = ld16<SC1>(rws, go[i]);
      u32 pend = OOB;
#pragma unroll
      for (int i = 0; i < 4; ++i) {
        const bool ok = (gr[i].y == tag) & (gr[i].w == tag) & (go[i] != OOB);
        const u32 ka = ok ? gr[i].x : 0u, kb = ok ? gr[i].z : 0u;
        k2 = umax(med3u(k1, k2, ka), kb); k1 = umax(k1, ka);
        go[i] = ok ? OOB : go[i];
        pend = umin(pend, go[i]);
      }
      if (wave_all(pend == OOB)) break;
      __builtin_amdgcn_s_sleep(SLP_REC);
    }
  }
  STAMP(4);
  const u32 t1 = wave_max(k1);
  const u32 t2 = wave_max(k1 == t1 ? k2 : k1);
  const u32 idx0 = 2047u - (t1 & 0x7FFu), idx1 = 2047u - (t2 & 0x7FFu);
  const u32 myrow = (lane >> 5) ? idx1 : idx0;
  const u32 so = WS_REC + (myrow >> 3) * 128 + 16 + (myrow & 7) * 8;
  const v4u xs = ld16<0>(rq, myrow * 2048 + (j & 3) * 512 + (lane & 31) * 16);
  v2u sg = ld8<SC1>(rws, so);
  v4u stt[16];
#pragma unroll
  for (int i = 0; i < 16; ++i) stt[i] = ld16<SC1>(rws, WS_REC + (i * 16 + (lane >> 2)) * 128 + 16 + (lane & 3) * 16);
  st4<SC1>(tag, rws, (L == 0 && lane == 0) ? WS_EPOCH : OOB);
  SCHED();
  if (!wave_all(sg.y == tag)) {
    SPIN(SPIN_G) {
      MEMFENCE();
      sg = ld8<SC1>(rws, so);
      if (wave_all(sg.y == tag)) break;
    }
  }
  {
    const float mu = lo2f(sg.x), rs = __builtin_amdgcn_rcpf(hi2f(sg.x));
    v4u xn;
#pragma unroll
    for (int i = 0; i < 4; ++i) { const u32 xx = xs[i]; xn[i] = pk2((lo2f(xx) - mu) * rs, (hi2f(xx) - mu) * rs); }
    lds_st<v4u>(L_XS + lane * 16, xn);
    MEMFENCE();
    lds_stv<v4u>(C_SEL, (v4u){1u, t1, t2, 0u});
    MEMFENCE();
  }
  STAMP(5);
  SPIN(SPIN_G) {
    bool ok = true;
#pragma unroll
    for (int i = 0; i < 16; ++i) ok = ok & (stt[i].y == tag) & (stt[i].w == tag);
    if (wave_all(ok)) break;
    MEMFENCE();
#pragma unroll
    for (int i = 0; i < 16; ++i) stt[i] = ld16<SC1>(rws, WS_REC + (i * 16 + (lane >> 2)) * 128 + 16 + (lane & 3) * 16);
  }
  lds_wait_min4(C_PUB, 0);
#pragma unroll
  for (int i = 0; i < 16; ++i) {
    const float r0 = __builtin_amdgcn_rcpf(hi2f(stt[i].x)), r1 = __builtin_amdgcn_rcpf(hi2f(stt[i].z));
    lds_st<f32x2>(L_RS + i * 512 + lane * 8, (f32x2){r0, r1});
    lds_st<f32x2>(L_NB + i * 512 + lane * 8, (f32x2){-lo2f(stt[i].x) * r0, -lo2f(stt[i].z) * r1});
  }
  MEMFENCE();
  lds_stv<u32>(C_STA, 1u);
  MEMFENCE();
  STAMP(6);
  {
    const rsrc_t ro = mkrs(p.out, S * D * 2);
    u32 ra = L_OS + ((lane >> 2) & 3) * OS_CH + ((lane >> 4) * 16 + (lane & 3) * 4) * 2;
    u32 row = lane;
#pragma unroll 1
    for (u32 c = 0; c < 8; ++c) {
      lds_wait_min4(C_OUT, c);
      v2u o[4];
#pragma unroll
      for (int i = 0; i < 4; ++i) o[i] = __builtin_bit_cast(v2u, __builtin_amdgcn_ds_read_tr16_b64_v4i16((LAS s16x4*)(size_t)(ra + i * 128)));
#pragma unroll
      for (int i = 0; i < 4; ++i) {
        const u32 r = row + i * 64;
        st8<OAUX>(o[i], ro, ((r == idx0) | (r == idx1)) ? OOB : r * 2048 + L * 8);
        if (SLP_ST) { SCHED(); __builtin_amdgcn_s_sleep(SLP_ST); SCHED(); }
      }
      ra += 512; row += 256;
    }
  }
  STAMP(7);
  STAMP_FLUSH();
}

__global__ void tiny(u32* w) { if (threadIdx.x == 0) w[512 + blockIdx.x] = now(); }
extern "C" void kernel_launch(void* const* d_in, const int* in_sizes, int n_in, void* d_out, int out_size, void* d_ws,
                              size_t ws_size, hipStream_t stream) {
  Params p;
  p.q = (const u16*)d_in[0]; p.k = (const u16*)d_in[1]; p.v = (const u16*)d_in[2];
  p.shk = (const u16*)d_in[3]; p.ssmk = (const u16*)d_in[4]; p.ssmD = (const u16*)d_in[5];
  p.wr = (const u16*)d_in[7]; p.wg = (const u16*)d_in[8]; p.wu = (const u16*)d_in[9]; p.wd = (const u16*)d_in[10];
  p.out = (u16*)d_out; p.ws = (char*)d_ws;
  (void)hipFuncSetAttribute((const void*)mega, hipFuncAttributeMaxDynamicSharedMemorySize, L_END);
  hipLaunchKernelGGL(mega, dim3(256), dim3(512), L_END, stream, p);
}
```

```cpp
#define POL_W 2
#include <hip/hip_runtime.h>
#include <stdint.h>
#include <utility>

typedef unsigned short u16;
typedef unsigned u32;
typedef unsigned long long u64;
typedef u32 v4u __attribute__((ext_vector_type(4)));
typedef u32 v2u __attribute__((ext_vector_type(2)));
typedef float f32x4 __attribute__((ext_vector_type(4)));
typedef float f32x2 __attribute__((ext_vector_type(2)));
typedef __bf16 bf16x8 __attribute__((ext_vector_type(8)));
typedef __bf16 bf16x2 __attribute__((ext_vector_type(2)));
typedef short s16x4 __attribute__((ext_vector_type(4)));
#define LAS __attribute__((address_space(3)))
#define CAS __attribute__((address_space(4)))
typedef __amdgpu_buffer_rsrc_t rsrc_t;

#define GAS __attribute__((address_space(1)))
#define POL_IN 0
#define WIN 8
#define NRES 5
#define PD 4
#define CODE_KB 34
#define SC1 16
#define T_S1 130
#define T_S2 170
#define T_S3 210
#define T_W0 300
#define T_WS 8
#define T_REC 400
#define T_KV0 0
#define T_KVS 0
#define T_2A 50
#define T_2B 50
#define T_NF 700
#define T_Y 80
#define T_Y3 40
#define T_KVP 60
#define T_H 100
#define LNPRIO 1
#define RFIX 4000
#define SLP_REC 6
#define SLP_KV 4

constexpr int S = 2048, D = 1024, F = 3584;
constexpr u32 SENT = 0xFFFFFFFFu;
constexpr u32 OOB = 0x80000000u;
constexpr u32 SPIN_G = 1u << 16;
constexpr u32 SPIN_L = 1u << 22;

constexpr u32 WS_EPOCH = 0;
constexpr u32 WS_REC = 4096;
constexpr u32 WS_KVF = WS_REC + 32768;
constexpr u32 WS_YF = WS_KVF + 262144;
constexpr u32 WS_GU = WS_YF + 98304;
constexpr u32 WS_DN = 1u << 20;
constexpr u32 WS_KX = 2u << 20;
constexpr u32 YSTR = 35072;
constexpr u32 WS_YX = 6u << 20;
constexpr u32 WS_DBG = 11u << 20;
constexpr u32 WS_END = 12u << 20;

constexpr u32 HC_COPY = 4160, HC_CH = 4 * HC_COPY;
constexpr u32 L_HC = 0;
constexpr u32 UB_CH = 512 + 4096 + 64;
constexpr u32 L_UB = L_HC + 4 * HC_CH;
constexpr u32 OS_CH = 4224;
constexpr u32 L_OS = L_UB + 4 * UB_CH;
constexpr u32 XT_P = 2112;
constexpr u32 L_XT = L_OS + 4 * OS_CH;
constexpr u32 L_TS = L_XT + 8 * XT_P;
constexpr u32 L_XS = L_TS + 4 * 4224;
constexpr u32 L_HA = L_XS + 1024;
constexpr u32 L_DS = L_HA + 512;
constexpr u32 L_LN = L_DS + 2048;
constexpr u32 L_RN = L_LN + 64;
constexpr u32 L_CTL = L_RN + 64;
constexpr u32 C_OUT = L_CTL;
constexpr u32 C_SEL = L_CTL + 16;
constexpr u32 C_LN = L_CTL + 32;
constexpr u32 C_PUB = L_CTL + 48;
constexpr u32 C_STG = L_CTL + 64;
constexpr u32 C_YP = L_CTL + 68;
constexpr u32 C_TAG = L_CTL + 72;
constexpr u32 C_ZERO = L_CTL + 80;
constexpr u32 C_YOK = L_CTL + 96;
constexpr u32 C_TKT = L_CTL + 100;
constexpr u32 L_END = L_CTL + 128;
static_assert(L_END <= 163840 && L_END > 81920, "");

struct Params {
  const u16 *q, *k, *v, *shk, *ssmk, *ssmD, *wr, *wg, *wu, *wd;
  u16* out;
  char* ws;
};

__device__ __forceinline__ rsrc_t mkrs(const void* p, u32 bytes) {
  return __builtin_amdgcn_make_buffer_rsrc((void*)p, 0, bytes, 0x00020000);
}
template <int AUX> __device__ __forceinline__ v4u ld16(rsrc_t r, u32 off) { return __builtin_amdgcn_raw_buffer_load_b128(r, off, 0, AUX); }
template <int AUX> __device__ __forceinline__ v2u ld8(rsrc_t r, u32 off) { return __builtin_amdgcn_raw_buffer_load_b64(r, off, 0, AUX); }
template <int AUX> __device__ __forceinline__ u32 ld4(rsrc_t r, u32 off) { return __builtin_amdgcn_raw_buffer_load_b32(r, off, 0, AUX); }
template <int AUX> __device__ __forceinline__ void st16(v4u v, rsrc_t r, u32 off) { __builtin_amdgcn_raw_buffer_store_b128(v, r, off, 0, AUX); }
template <int AUX> __device__ __forceinline__ void st8(v2u v, rsrc_t r, u32 off) { __builtin_amdgcn_raw_buffer_store_b64(v, r, off, 0, AUX); }
template <int AUX> __device__ __forceinline__ void st4(u32 v, rsrc_t r, u32 off) { __builtin_amdgcn_raw_buffer_store_b32(v, r, off, 0, AUX); }

__device__ __forceinline__ float asf(u32 x) { return __builtin_bit_cast(float, x); }
__device__ __forceinline__ u32 asu(float x) { return __builtin_bit_cast(u32, x); }
__device__ __forceinline__ float lo2f(u32 x) { return asf(x << 16); }
__device__ __forceinline__ float hi2f(u32 x) { return asf(x & 0xFFFF0000u); }
__device__ __forceinline__ u32 pk2(float lo, float hi) {
  f32x2 f = {lo, hi};
  return __builtin_bit_cast(u32, __builtin_convertvector(f, bf16x2));
}
__device__ __forceinline__ float rbf(float f) { return lo2f(pk2(f, f)); }
__device__ __forceinline__ float dot2(u32 a, u32 b, float c) {
  return __builtin_amdgcn_fdot2_f32_bf16(__builtin_bit_cast(bf16x2, a), __builtin_bit_cast(bf16x2, b), c, false);
}
__device__ __forceinline__ f32x4 mfma(v4u a, v4u b, f32x4 c) {
  return __builtin_amdgcn_mfma_f32_16x16x32_bf16(__builtin_bit_cast(bf16x8, a), __builtin_bit_cast(bf16x8, b), c, 0, 0, 0);
}
#define SCHED() __builtin_amdgcn_sched_barrier(0)
#define VMCNT(n) do { SCHED(); asm volatile("s_waitcnt vmcnt(%0)" ::"n"(n) : "memory"); \
    __builtin_amdgcn_s_waitcnt(((n) & 15) | (7 << 4) | (15 << 8) | (((n) >> 4) << 14)); SCHED(); } while (0)
#define LGKM0() asm volatile("s_waitcnt lgkmcnt(0)" ::: "memory")
#define MEMFENCE() asm volatile("" ::: "memory")
#define SPIN(cap) _Pragma("unroll 1") for (u32 spins = 0; spins < (cap); ++spins)

template <int CTRL, int RM> __device__ __forceinline__ float dppf(float x) {
  return asf(__builtin_amdgcn_update_dpp(0u, asu(x), CTRL, RM, 0xf, false));
}
template <int CTRL, int RM> __device__ __forceinline__ u32 dppu(u32 x) { return __builtin_amdgcn_update_dpp(0u, x, CTRL, RM, 0xf, false); }
__device__ __forceinline__ float wave_sum63(float x) {
  x += dppf<0x111, 0xf>(x); x += dppf<0x112, 0xf>(x); x += dppf<0x114, 0xf>(x); x += dppf<0x118, 0xf>(x);
  x += dppf<0x142, 0xa>(x); x += dppf<0x143, 0xc>(x);
  return x;
}
__device__ __forceinline__ u32 umax(u32 a, u32 b) { return a > b ? a : b; }
__device__ __forceinline__ u32 umin(u32 a, u32 b) { return a < b ? a : b; }
__device__ __forceinline__ u32 wave_max(u32 x) {
  x = umax(x, dppu<0x111, 0xf>(x)); x = umax(x, dppu<0x112, 0xf>(x)); x = umax(x, dppu<0x114, 0xf>(x)); x = umax(x, dppu<0x118, 0xf>(x));
  x = umax(x, dppu<0x142, 0xa>(x)); x = umax(x, dppu<0x143, 0xc>(x));
  return __builtin_amdgcn_readlane(x, 63);
}
__device__ __forceinline__ u32 med3u(u32 a, u32 b, u32 c) {
  u32 r; asm("v_med3_u32 %0, %1, %2, %3" : "=v"(r) : "v"(a), "v"(b), "v"(c)); return r;
}
__device__ __forceinline__ bool wave_all(bool p) { return __builtin_amdgcn_ballot_w64(p) == ~0ull; }
__device__ __forceinline__ u32 nosent(float f) { return umin(asu(f), SENT - 1u); }
__device__ __forceinline__ u32 rfl(u32 x) { return __builtin_amdgcn_readfirstlane(x); }

template <typename T> __device__ __forceinline__ T lds_ld(u32 a) { return *(LAS T*)(size_t)a; }
template <typename T> __device__ __forceinline__ T lds_ldv(u32 a) { return *(volatile LAS T*)(size_t)a; }
template <typename T> __device__ __forceinline__ void lds_st(u32 a, T v) { *(LAS T*)(size_t)a = v; }
template <typename T> __device__ __forceinline__ void lds_stv(u32 a, T v) { *(volatile LAS T*)(size_t)a = v; }
__device__ __forceinline__ void lds_wait_nz(u32 a) {
  SPIN(SPIN_L) {
    if (rfl(lds_ldv<u32>(a))) break;
    __builtin_amdgcn_s_sleep(1);
  }
  MEMFENCE();
}
__device__ __forceinline__ void lds_wait_min4(u32 a, u32 gt) {
  SPIN(SPIN_L) {
    const v4u f = lds_ldv<v4u>(a);
    if (rfl(umin(umin(f.x, f.y), umin(f.z, f.w))) > gt) break;
    __builtin_amdgcn_s_sleep(1);
  }
  MEMFENCE();
}

#define STAMP(i)
#define STAMP_DECL
#define STAMP_FLUSH()

__device__ __forceinline__ void perm8(v4u* w) {
  v4u o[8];
#pragma unroll
  for (int c = 0; c < 8; ++c)
#pragma unroll
    for (int i = 0; i < 4; ++i)
      o[c][i] = __builtin_amdgcn_perm(w[2 * i + 1][c >> 1], w[2 * i][c >> 1], (c & 1) ? 0x07060302u : 0x05040100u);
#pragma unroll
  for (int c = 0; c < 8; ++c) w[c] = o[c];
}

__device__ __forceinline__ v4u rdA(u32 aA, int m) {
  v2u lo = lds_ldv<v2u>(aA + 64 * m), hi = lds_ldv<v2u>(aA + 64 * m + 8);
  return (v4u){lo.x, lo.y, hi.x, hi.y};
}

constexpr int NSTEP = 160, RING = PD + 2;
constexpr int st_pp(int s) { return s < 16 ? 0 : s < 48 ? 1 : s < 96 ? 2 : 3; }
constexpr int st_b0(int pp) { return pp == 0 ? 0 : pp == 1 ? 16 : pp == 2 ? 48 : 96; }
constexpr int st_ls(int s) { return s - st_b0(st_pp(s)); }
constexpr bool st_diag(int s) { return st_ls(s) >= 16 * st_pp(s); }
constexpr int st_m0(int s) { return st_diag(s) ? (st_ls(s) - 16 * st_pp(s)) / 2 : st_ls(s) / (2 * st_pp(s)); }
constexpr int st_bb(int s) { return st_diag(s) ? 2 * st_pp(s) + (st_ls(s) & 1) : st_ls(s) % (2 * st_pp(s)); }
constexpr bool st_needP(int s) { return s < NSTEP && st_bb(s) == 0 && 2 * st_pp(s) + 1 >= NRES; }
constexpr bool st_needA(int s) { return s < NSTEP && 2 * st_pp(s) - st_bb(s) >= NRES; }

__device__ __forceinline__ void mfma_acc(f32x4& c, v4u a, v4u b) {
  asm volatile("v_mfma_f32_16x16x32_bf16 %0, %1, %2, %0" : "+v"(c) : "v"(a), "v"(b));
}
__device__ __forceinline__ void mfma_one(f32x4& c, v4u a, v4u b) {
  asm volatile("v_mfma_f32_16x16x32_bf16 %0, %1, %2, 1.0" : "=&v"(c) : "v"(a), "v"(b));
}

__device__ __forceinline__ v2u pkmin(v2u o) {
  typedef unsigned short h2 __attribute__((ext_vector_type(2)));
  const h2 m = {0xFFFE, 0xFFFE};
  const u32 x = o.x, y = o.y;
  return (v2u){__builtin_bit_cast(u32, __builtin_elementwise_min(__builtin_bit_cast(h2, x), m)), __builtin_bit_cast(u32, __builtin_elementwise_min(__builtin_bit_cast(h2, y), m))};
}

struct Conv {
  v4u A[8 * NRES], AD, Bq[RING];
  v2u Al[RING], Ah[RING], Pl[2], Ph[2];
  f32x4 acc[2][2];
  u32 aA, aB, osw, flg, pk;

  template <int s> __device__ __forceinline__ void rdB() {
    if constexpr (s < NSTEP) Bq[s % RING] = lds_ldv<v4u>(aB + (8 * st_bb(s) - st_m0(s) + 7) * 64);
  }
  template <int s, int h> __device__ __forceinline__ void rdAq() {
    if constexpr (st_needA(s)) {
      constexpr int m = st_m0(s) + 8 * (2 * st_pp(s) - st_bb(s));
      if constexpr (h == 0) Al[s % RING] = lds_ldv<v2u>(aA + 64 * m); else Ah[s % RING] = lds_ldv<v2u>(aA + 64 * m + 8);
    }
  }
  template <int s, int h> __device__ __forceinline__ void rdP() {
    if constexpr (st_needP(s)) {
      constexpr int m = st_m0(s) + 8 * (2 * st_pp(s) + 1);
      if constexpr (h == 0) Pl[st_m0(s) & 1] = lds_ldv<v2u>(aA + 64 * m); else Ph[st_m0(s) & 1] = lds_ldv<v2u>(aA + 64 * m + 8);
    }
  }
  template <int s> __device__ __forceinline__ v4u ringA() {
    const v2u l = Al[s % RING], h = Ah[s % RING];
    return (v4u){l.x, l.y, h.x, h.y};
  }
  template <int pp, int e> __device__ __forceinline__ void fin() {
    const f32x4 a = acc[pp & 1][e];
    v2u o = {pk2(a[0], a[1]), pk2(a[2], a[3])};
    if constexpr (pp == 3) o = pkmin(o);
    lds_stv<v2u>(osw + (2 * pp + e) * 512, o);
    if constexpr (e == 1) lds_stv<u32>(flg, pp + 1);
  }
  template <int s> __device__ __forceinline__ void step() {
    constexpr int pp = st_pp(s), m0 = st_m0(s), bb = st_bb(s), c0 = 2 * pp, c1 = c0 + 1, ls = st_ls(s);
    if constexpr (pp > 0 && ls == 16 * pp - PD - 3) pk = lds_ldv<u32>(C_STG);
    if constexpr (pp > 0 && ls == 16 * pp - PD) {
      if (rfl(pk) <= (u32)pp) {
        SPIN(SPIN_L) { if (rfl(lds_ldv<u32>(C_STG)) > (u32)pp) break; __builtin_amdgcn_s_sleep(1); }
      }
    }
    const v4u Bf = Bq[s % RING];
    v4u A1, A0;
    if constexpr (c1 - bb < NRES) A1 = A[m0 + 8 * (c1 - bb)];
    else if constexpr (bb == 0) A1 = (v4u){Pl[m0 & 1].x, Pl[m0 & 1].y, Ph[m0 & 1].x, Ph[m0 & 1].y};
    else A1 = ringA<s - 1>();
    if constexpr (bb <= c0) {
      if constexpr (c0 - bb < NRES) A0 = A[m0 + 8 * (c0 - bb)]; else A0 = ringA<s>();
    }
    if constexpr (st_needA(s)) asm volatile("" :: "v"(Ah[s % RING]));
    if constexpr (ls == 0) mfma_one(acc[pp & 1][1], A1, Bf); else mfma_acc(acc[pp & 1][1], A1, Bf);
    rdB<s + PD>();
    rdAq<s + PD, 0>();
    rdP<s + PD + 1, 0>();
    SCHED();
    if constexpr (bb <= c0) {
      if constexpr (ls == 0) mfma_one(acc[pp & 1][0], A0, Bf); else mfma_acc(acc[pp & 1][0], A0, Bf);
    }
    rdAq<s + PD, 1>();
    rdP<s + PD + 1, 1>();
    if constexpr (pp > 0 && ls == 2) fin<pp - 1, 0>();
    if constexpr (pp > 0 && ls == 3) fin<pp - 1, 1>();
    if constexpr (m0 == 0 && bb == c1) mfma_acc(acc[pp & 1][1], AD, Bf);
    if constexpr (m0 == 0 && bb == c0) mfma_acc(acc[pp & 1][0], AD, Bf);
    SCHED();
  }
  template <int... Is> __device__ __forceinline__ void pre(std::integer_sequence<int, Is...>) { (rdB<Is>(), ...); }
  template <int... Is> __device__ __forceinline__ void run(std::integer_sequence<int, Is...>) { (step<Is>(), ...); }
  __device__ __forceinline__ void stream() {
    pre(std::make_integer_sequence<int, PD>{});
    run(std::make_integer_sequence<int, NSTEP>{});
    asm volatile("s_nop 7" : "+v"(acc[1][0]), "+v"(acc[1][1]));
    fin<3, 0>();
    fin<3, 1>();
  }
  __device__ __forceinline__ void setup(int w, int lane, u32 Dbits) {
    const int M = lane & 15, q4 = lane >> 4;
    const u32 ub = L_UB + w * UB_CH, hb = L_HC + w * HC_CH;
    aA = hb + ((M + 1) & 3) * HC_COPY + ((M + 1) >> 2) * 8 + q4 * 16;
#pragma unroll
    for (int m = 0; m < 8 * NRES; ++m) A[m] = rdA(aA, m);
    const int jd = 15 - M - 8 * q4;
#pragma unroll
    for (int i = 0; i < 4; ++i) AD[i] = jd == 2 * i ? Dbits : jd == 2 * i + 1 ? (Dbits << 16) : 0u;
    aB = ub + 512 - 7 * 64 + (2 * M + 1 - q4) * 16;
    osw = L_OS + w * OS_CH + (16 * M + 4 * q4) * 2;
    flg = C_OUT + w * 4;
    asm volatile("" : "+v"(aB), "+v"(aA), "+v"(flg), "+v"(osw), "+v"(AD));
#pragma unroll
    for (int m = 0; m < 8 * NRES; ++m) asm volatile("" : "+v"(A[m]));
  }
};

__device__ __forceinline__ u32 get_tag() {
  u32 tag = 0;
  SPIN(SPIN_L) {
    const v2u f = lds_ldv<v2u>(C_TAG);
    tag = rfl(f.y);
    if (rfl(f.x)) break;
    __builtin_amdgcn_s_sleep(1);
  }
  return tag;
}

typedef unsigned short u16x2 __attribute__((ext_vector_type(2)));
__device__ __forceinline__ v4u chk_consts(int lane) {
  const u32 m = (u32)(lane & 15) * 0x1E3A2B46u;
  v4u k = {(0x9E37B5C3u + m) | 0x00010001u, (0xC2B3AE3Du ^ m) | 0x00010001u, (0x85EBF4A7u - m) | 0x00010001u, (0xD35B6F19u + 3u * m) | 0x00010001u};
  asm volatile("" : "+v"(k));
  return k;
}
__device__ __forceinline__ u32 chk_piece(v4u d, v4u k, u32 a) {
#pragma unroll
  for (int e = 0; e < 4; ++e) {
    const u32 x = d[e], c = k[e];
    a = __builtin_amdgcn_udot2(__builtin_bit_cast(u16x2, x), __builtin_bit_cast(u16x2, c), a, false);
  }
  return a;
}
#define ROWSUM(ctl) "v_add_u32_dpp %0, %0, %0 " ctl " row_mask:0xf bank_mask:0xf\n\tv_add_u32_dpp %1, %1, %1 " ctl " row_mask:0xf bank_mask:0xf\n\t" \
                    "v_add_u32_dpp %2, %2, %2 " ctl " row_mask:0xf bank_mask:0xf\n\tv_add_u32_dpp %3, %3, %3 " ctl " row_mask:0xf bank_mask:0xf\n\t"
__device__ __forceinline__ u32 chk_make(const v4u* d, v4u k, int lane) {
  u32 h[4];
#pragma unroll
  for (int i = 0; i < 4; ++i) h[i] = chk_piece(d[i], k, 0u);
  asm("s_nop 2\n\t" ROWSUM("row_ror:1") ROWSUM("row_ror:2") ROWSUM("row_ror:4") ROWSUM("row_ror:8") : "+v"(h[0]), "+v"(h[1]), "+v"(h[2]), "+v"(h[3]));
  const u32 lo = (lane & 1) ? h[1] : h[0], hi = (lane & 1) ? h[3] : h[2];
  return (lane & 2) ? hi : lo;
}
#undef ROWSUM
__device__ __forceinline__ bool chk_ok(const v4u* d, v4u k, u32 neg) {
  u32 a = chk_piece(d[1], k, chk_piece(d[0], k, neg)) + chk_piece(d[3], k, chk_piece(d[2], k, 0u));
  a += dppu<0x121, 0xf>(a); a += dppu<0x122, 0xf>(a); a += dppu<0x124, 0xf>(a); a += dppu<0x128, 0xf>(a);
  return wave_all(a == 0u);
}

__device__ __forceinline__ u32 now() { return (u32)__builtin_amdgcn_s_memrealtime(); }
__device__ __forceinline__ void hold(u32 t0, u32 dt) {
  SPIN(4096) { if (now() - t0 >= dt) break; __builtin_amdgcn_s_sleep(1); }
}

__device__ __forceinline__ void epilogue(rsrc_t rws, rsrc_t ro, u32 row0, int lane) {
  SPIN(4) {
    u32 qd = 0;
    if (lane == 0) qd = __hip_atomic_fetch_add((LAS u32*)(size_t)C_TKT, 1u, __ATOMIC_RELAXED, __HIP_MEMORY_SCOPE_WORKGROUP);
    qd = rfl(qd);
    if (qd >= 4) break;
    const u32 xa = L_XT + (lane & 3) * XT_P + qd * 512 + (lane >> 2) * 8;
    u32 yo = WS_YX + (row0 >> 4) * YSTR + (qd * 64 + (lane >> 2)) * 128 + (row0 & 8) * 8 + (lane & 3) * 16;
    float xf[4][8], xn[4][8];
    {
      const f32x2 s0 = lds_ld<f32x2>(L_RN + (lane & 3) * 8), s1 = lds_ld<f32x2>(L_RN + 32 + (lane & 3) * 8);
#pragma unroll
      for (int i = 0; i < 4; ++i) {
        const v2u a = lds_ld<v2u>(xa + i * 128), b = lds_ld<v2u>(xa + i * 128 + 4 * XT_P);
        xf[i][0] = lo2f(a.x); xf[i][1] = hi2f(a.x); xf[i][2] = lo2f(a.y); xf[i][3] = hi2f(a.y);
        xf[i][4] = lo2f(b.x); xf[i][5] = hi2f(b.x); xf[i][6] = lo2f(b.y); xf[i][7] = hi2f(b.y);
#pragma unroll
        for (int e = 0; e < 8; ++e) {
          xn[i][e] = e < 4 ? __builtin_fmaf(xf[i][e], s0.x, s0.y) : __builtin_fmaf(xf[i][e], s1.x, s1.y);
          asm volatile("" : "+v"(xf[i][e]), "+v"(xn[i][e]));
        }
      }
    }
    asm volatile("" : "+v"(yo));
    lds_wait_nz(C_YOK);
    v4u y[4];
#pragma unroll
    for (int i = 0; i < 4; ++i) y[i] = ld16<SC1>(rws, yo + i * 2048);
    const v4u ks4 = lds_ldv<v4u>(C_SEL);
    const u32 i0 = 2047u - (ks4.y & 0x7FFu), i1 = 2047u - (ks4.z & 0x7FFu);
    SCHED();
    if (row0 >= 1536) SPIN(SPIN_G) {
      u32 mx[4];
#pragma unroll
      for (int i = 0; i < 4; ++i) mx[i] = umax(umax(y[i].x, y[i].y), umax(y[i].z, y[i].w));
      if (wave_all(umax(umax(mx[0], mx[1]), umax(mx[2], mx[3])) != SENT)) break;
      __builtin_amdgcn_s_sleep(SLP_KV);
      MEMFENCE();
#pragma unroll
      for (int i = 0; i < 4; ++i) if (mx[i] == SENT) y[i] = ld16<SC1>(rws, yo + i * 2048);
    }
#pragma unroll
    for (int i = 0; i < 4; ++i) {
      u32 o[4];
#pragma unroll
      for (int d = 0; d < 4; ++d) {
        const u32 yy = y[i][d];
        o[d] = pk2(__builtin_fmaf(xn[i][2 * d], lo2f(yy), xf[i][2 * d]), __builtin_fmaf(xn[i][2 * d + 1], hi2f(yy), xf[i][2 * d + 1]));
      }
      lds_st<v2u>(xa + i * 128, (v2u){o[0], o[1]});
      lds_st<v2u>(xa + i * 128 + 4 * XT_P, (v2u){o[2], o[3]});
    }
    MEMFENCE();
#pragma unroll
    for (int i = 0; i < 4; ++i) {
      const u32 r = row0 + 2 * i + (lane >> 5);
      const v4u t = lds_ld<v4u>(L_XT + (2 * i + (lane >> 5)) * XT_P + qd * 512 + (lane & 31) * 16);
      st16<0>(t, ro, ((r == i0) | (r == i1)) ? OOB : r * 2048 + qd * 512 + (lane & 31) * 16);
    }
  }
}

typedef u32 v16u __attribute__((ext_vector_type(16)));
typedef u32 v8u __attribute__((ext_vector_type(8)));
#define ENDW() do { STAMP_FLUSH(); asm volatile("s_endpgm" ::: "memory"); } while (0)

__global__ void __launch_bounds__(512, 2) mega(Params p_unused) {
  extern __shared__ __attribute__((aligned(16))) char smem[];
  u64 pc0;
  v16u ka;
  v8u kb;
  u64 t64;
  asm volatile("s_load_dwordx16 %1, %4, 0x0\n\ts_load_dwordx8 %2, %4, 0x40\n\ts_memrealtime %3\n\ts_getpc_b64 %0"
               : "=&s"(pc0), "=&s"(ka), "=&s"(kb), "=&s"(t64) : "s"(__builtin_amdgcn_kernarg_segment_ptr()));
  const int tid = threadIdx.x;
  int wave = __builtin_amdgcn_readfirstlane(tid >> 6);
  const int lane = tid & 63;
  const int b = blockIdx.x, xcd = b & 7, rk = b >> 3;
  const u32 codepf = ld4<0>(mkrs((const void*)(pc0 & ~255ull), CODE_KB * 1024), (rk & 3) * 10240 + wave * 1280 + lane * 128 + (lane >= 10 ? OOB : 0u));
  SCHED();
  const int L = xcd * 32 + rk;
  lds_st<u32>(L_CTL + (tid & 31) * 4, 0u);
  const int jw = wave & 3, cg = L >> 4, rb = L & 15, ch = 4 * L + jw;
  const u32 row0 = (u32)((((L >> 1) & 3) * 32 + (L >> 3)) * 16 + (L & 1) * 8);
  u32 toff = (u32)((jw * 512 + rb * 32 + (lane >> 3) * 4) * 2048 + cg * 128 + (lane & 7) * 16);
  u32 qoff = (row0 + 2 * jw + (lane >> 5)) * 2048 + (lane & 31) * 16;
  asm volatile("" : "+v"(toff), "+v"(qoff));
  asm volatile("s_waitcnt lgkmcnt(0)" : "+s"(ka), "+s"(kb), "+s"(t64) :: "memory");
  const u32 t0 = (u32)t64;
  STAMP_DECL;
  __builtin_amdgcn_s_barrier();
  SCHED();
  Params p;
#define KP(v, i) (((u64)v[2 * (i) + 1] << 32) | v[2 * (i)])
  p.q = (const u16*)KP(ka, 0); p.k = (const u16*)KP(ka, 1); p.v = (const u16*)KP(ka, 2); p.shk = (const u16*)KP(ka, 3);
  p.ssmk = (const u16*)KP(ka, 4); p.ssmD = (const u16*)KP(ka, 5); p.wr = (const u16*)KP(ka, 6); p.wg = (const u16*)KP(ka, 7);
  p.wu = (const u16*)KP(kb, 0); p.wd = (const u16*)KP(kb, 1); p.out = (u16*)KP(kb, 2); p.ws = (char*)KP(kb, 3);
#undef KP
  const rsrc_t rq = mkrs(p.q, S * D * 2);
  v4u U[19];
  if (wave >= 4) {
#pragma unroll
    for (int i = 0; i < 4; ++i) U[i] = ld16<POL_IN>(rq, qoff + i * 512);
#pragma unroll
    for (int i = 0; i < 4; ++i) U[4 + i] = ld16<0>(mkrs(p.wr, D * 2), (lane & 31) * 16 + i * 512);
    SCHED();
  }
  asm volatile("" : "+s"(wave));
  if (wave < 4) {
    const rsrc_t rk_ = mkrs(p.k, S * D * 2), rv = mkrs(p.v, S * D * 2), rs = mkrs(p.shk, D * 8);
    if (jw) { SCHED(); hold(t0, T_H); }
#pragma unroll
    for (int i = 0; i < 4; ++i) U[15 + i] = ld16<POL_IN>(mkrs(p.ssmk, S * D * 2), ch * 4096 + (i * 64 + lane) * 16);
    SCHED();
    if (jw) { SCHED(); hold(t0, jw == 1 ? T_S1 : jw == 2 ? T_S2 : T_S3); }
#pragma unroll
    for (int i = 0; i < 4; ++i) U[3 + i] = ld16<POL_IN>(rk_, toff + i * 2048);
#pragma unroll
    for (int i = 0; i < 4; ++i) U[7 + i] = ld16<POL_IN>(rv, toff + i * 2048);
#pragma unroll
    for (int i = 0; i < 3; ++i) U[i] = ld16<0>(rk_, toff - (3 - i) * 2048);
#pragma unroll
    for (int i = 0; i < 4; ++i) U[11 + i] = ld16<0>(rs, (cg * 64 + (lane & 7) * 8) * 8 + i * 16);
    SCHED();
  }
  asm volatile("" : "+s"(wave));
  const rsrc_t rws = mkrs(p.ws, WS_END);
  const bool ffn = rk < 28;
  const int j = xcd * 28 + rk;
  u32 epoch = 0;
  if (wave >= 4) {
    epoch = *(const CAS u32*)(p.ws + WS_EPOCH);
    const v4u sent = {SENT, SENT, SENT, SENT};
    st16<SC1>(sent, rws, WS_YX + (96 + jw * 8 + (lane >> 3)) * YSTR + L * 128 + (lane & 7) * 16);
    st16<SC1>(sent, rws, (!ffn || jw == 3) ? OOB : jw == 2 ? WS_GU + j * 1024 + lane * 16 : WS_DN + j * 2048 + jw * 1024 + lane * 16);
    SCHED();
    asm volatile("" : "+s"(epoch));
    if (jw == 3) lds_stv<v2u>(C_TAG, (v2u){1u, epoch + 1u});
  __builtin_amdgcn_s_setprio(LNPRIO);
  STAMP(1);
  {
    float sx = 0.f, sq = 0.f, sr = 0.f, sw = 0.f;
#pragma unroll
    for (int i = 0; i < 16; ++i) {
      const u32 a = U[i >> 2][i & 3], wv = U[4 + (i >> 2)][i & 3];
      sx = dot2(a, 0x3f803f80u, sx); sq = dot2(a, a, sq); sr = dot2(a, wv, sr); sw = dot2(wv, 0x3f803f80u, sw);
    }
#define RED4(ctl) "v_add_f32_dpp %0, %0, %0 " ctl "\n\tv_add_f32_dpp %1, %1, %1 " ctl "\n\tv_add_f32_dpp %2, %2, %2 " ctl "\n\tv_add_f32_dpp %3, %3, %3 " ctl "\n\t"
    asm("s_nop 2\n\t" RED4("row_shr:1 row_mask:0xf bank_mask:0xf bound_ctrl:1") RED4("row_shr:2 row_mask:0xf bank_mask:0xf bound_ctrl:1")
        RED4("row_shr:4 row_mask:0xf bank_mask:0xf bound_ctrl:1") RED4("row_shr:8 row_mask:0xf bank_mask:0xf bound_ctrl:1")
        RED4("row_bcast:15 row_mask:0xa bank_mask:0xf") : "+v"(sx), "+v"(sq), "+v"(sr), "+v"(sw));
#undef RED4
    const float mu = sx * (1.f / D);
    const u32 mv = pk2(mu, __builtin_fmaxf(__builtin_fmaf(-mu, mu, sq * (1.f / D)), 0.f));
    const float sd = rbf(__builtin_amdgcn_sqrtf(rbf(hi2f(mv) + 1e-5f)));
    const float rs = __builtin_amdgcn_rcpf(sd);
    const u32 rp = pk2(0.f, __builtin_fmaf(-lo2f(mv), sw, sr) * rs);
    u32 key = (rp ^ ((u32)((int)rp >> 31) | 0x80000000u)) & 0xFFFF0000u;
    key |= 2047u - (row0 + 2 * jw + (lane >> 5));
    u32 ms = (mv & 0xFFFFu) | asu(sd);
    asm volatile("" : "+v"(ms), "+v"(key) : "v"(codepf));
#pragma unroll
    for (int i = 0; i < 4; ++i) lds_st<v4u>(L_XT + (2 * jw + (lane >> 5)) * XT_P + (lane & 31) * 16 + i * 512, U[i]);
    VMCNT(0);
    if ((lane & 31) == 31) {
      lds_st<f32x2>(L_RN + (2 * jw + (lane >> 5)) * 8, (f32x2){rs, -lo2f(mv) * rs});
      lds_st<u32>(L_LN + jw * 16 + (lane >> 5) * 4, ms);
      lds_st<u32>(L_LN + jw * 16 + 8 + (lane >> 5) * 4, key);
      MEMFENCE();
      lds_stv<u32>(C_LN + jw * 4, 1u);
    }
    MEMFENCE();
  }
    __builtin_amdgcn_s_setprio(0);
    STAMP(2);
  }
  asm volatile("" : "+s"(wave));

  if (wave < 4) {
    const int w = jw;
    const int g = lane & 7, rr = lane >> 3;
    v2u hn[4];
    const u32 Dw = *(const CAS u32*)(p.ssmD + (ch & ~1));
    SCHED();
    const u32 hb = L_TS + w * 4224;
    u32 tw = hb + g * 528 + (rr >> 1) * 16 + ((rr & 1) ? 0 : 8), tr_ = hb + (lane >> 5) * 528 + (lane & 31) * 16;
    u32 ko = WS_KX + (cg * 64 + w * 16 + rb) * 4096 + lane * 16;
    asm volatile("" : "+v"(tw), "+v"(tr_), "+v"(ko));
    if (lane < 32) lds_st<v4u>(L_UB + w * UB_CH + lane * 16, (v4u){0, 0, 0, 0});
    auto copies = [&]() {
    SCHED();
    const u32 hc = L_HC + w * HC_CH;
#pragma unroll
    for (int i = 0; i < 4; ++i) {
      const u32 nx = i < 3 ? rfl(U[i < 3 ? 16 + i : 18].x) : 0u, ny = i < 3 ? rfl(U[i < 3 ? 16 + i : 18].y) : 0u;
      hn[i].x = __builtin_amdgcn_update_dpp(nx, U[15 + i].x, 0x130, 0xf, 0xf, false);
      hn[i].y = __builtin_amdgcn_update_dpp(ny, U[15 + i].y, 0x130, 0xf, 0xf, false);
    }
#pragma unroll
    for (int i = 0; i < 4; ++i) {
      const v4u d = U[15 + i];
      const u32 e0 = __builtin_amdgcn_alignbit(d.y, d.x, 16), e1 = __builtin_amdgcn_alignbit(d.z, d.y, 16),
                e2 = __builtin_amdgcn_alignbit(d.w, d.z, 16), e3 = __builtin_amdgcn_alignbit(hn[i].x, d.w, 16),
                e4 = __builtin_amdgcn_alignbit(hn[i].y, hn[i].x, 16);
      const u32 a = hc + 32 + (i * 64 + lane) * 16;
      lds_st<v4u>(a, d);
      lds_st<v4u>(a + HC_COPY, (v4u){e0, e1, e2, e3});
      lds_st<v4u>(a + 2 * HC_COPY, (v4u){d.y, d.z, d.w, hn[i].x});
      lds_st<v4u>(a + 3 * HC_COPY, (v4u){e1, e2, e3, e4});
    }
    {
      const u32 s0 = rfl(U[15].x), s1 = rfl(U[15].y);
      const int cb = lane & 3;
      const u32 w3 = cb == 0 ? 0u : cb == 1 ? (s0 << 16) : cb == 2 ? s0 : ((s0 >> 16) | (s1 << 16));
      const v4u z = {0, 0, lane == 7 ? (s0 << 16) : 0u, (lane & 4) ? w3 : 0u};
      if (lane < 8) lds_st<v4u>(hc + cb * HC_COPY + (lane >> 2) * 16, z);
    }
    MEMFENCE();
    SCHED();
    };
    const v4u kc = chk_consts(lane);
    if (w) copies();
    const u32 tag = get_tag();
    STAMP(1);
    VMCNT(0);
    asm volatile("" :: "v"(codepf));
    STAMP(2);
#pragma unroll
    for (int c = 0; c < 8; ++c) {
      const u32 wa = U[11 + (c >> 1)][(c & 1) * 2], wb = U[11 + (c >> 1)][(c & 1) * 2 + 1];
      const u32 sel = (c & 1) ? 0x07060302u : 0x05040100u;
      u32 P[6];
      float ks[4], vf[4];
#pragma unroll
      for (int i = 0; i < 6; ++i) P[i] = __builtin_amdgcn_perm(U[i][c >> 1], U[i + 1][c >> 1], sel);
#pragma unroll
      for (int i = 0; i < 4; ++i) { const u32 vx = U[7 + i][c >> 1]; vf[i] = (c & 1) ? hi2f(vx) : lo2f(vx); }
      asm("v_dot2_f32_bf16 %0, %4, %11, 0\n\tv_dot2_f32_bf16 %1, %5, %11, 0\n\tv_dot2_f32_bf16 %2, %6, %11, 0\n\tv_dot2_f32_bf16 %3, %7, %11, 0\n\t"
          "v_dot2_f32_bf16 %0, %6, %10, %0\n\tv_dot2_f32_bf16 %1, %7, %10, %1\n\tv_dot2_f32_bf16 %2, %8, %10, %2\n\tv_dot2_f32_bf16 %3, %9, %10, %3\n\t"
          "v_mul_f32 %0, %0, %12\n\tv_mul_f32 %1, %1, %13\n\tv_mul_f32 %2, %2, %14\n\tv_mul_f32 %3, %3, %15"
          : "=&v"(ks[0]), "=&v"(ks[1]), "=&v"(ks[2]), "=&v"(ks[3])
          : "v"(P[0]), "v"(P[1]), "v"(P[2]), "v"(P[3]), "v"(P[4]), "v"(P[5]), "v"(wa), "v"(wb), "v"(vf[0]), "v"(vf[1]), "v"(vf[2]), "v"(vf[3]));
      lds_st<v2u>(tw + c * 64, (v2u){pk2(ks[3], ks[2]), pk2(ks[1], ks[0])});
    }
    MEMFENCE();
    {
      v4u d[4];
#pragma unroll
      for (int i = 0; i < 4; ++i) d[i] = lds_ld<v4u>(tr_ + i * 1056);
#pragma unroll
      for (int i = 0; i < 4; ++i) st16<SC1>(d[i], rws, ko + i * 1024);
      SCHED();
      st4<SC1>(chk_make(d, kc, lane) + tag * 0x9E3779B1u, rws, WS_KVF + (cg * 64 + w * 16 + rb) * 256 + lane * 4);
      lds_stv<u32>(C_PUB + w * 4, now() | 1u);
    }
    MEMFENCE();
    STAMP(3);
    if (w == 0) copies();
    STAMP(4);
    SCHED();
    Conv cv;
    cv.setup(w, lane, (ch & 1) ? (Dw >> 16) : (Dw & 0xFFFFu));
    STAMP(5);
    lds_wait_nz(C_STG);
    STAMP(6);
    cv.stream();
    STAMP(7);
    epilogue(rws, mkrs(p.out, S * D * 2), row0, lane);
    STAMP(8);
    ENDW();
  }

  v4u R[48];
  const rsrc_t rw = mkrs(jw == 0 ? p.wg : p.wu, D * F * 2);
  const u32 wo = ((j & 3) * 256 + (lane >> 3) * 8) * (F * 2) + (j >> 2) * 128 + (lane & 7) * 16;

  if (jw < 2) {
    if (ffn) {
    const int q4 = lane >> 4, N = lane & 15;
    const rsrc_t rd = mkrs(p.wd, D * F * 2);
    const u32 dofs = ((j >> 2) * 64 + q4 * 8) * 2048 + (j & 3) * 512 + jw * 256 + N * 16;
#pragma unroll
    for (int i = 0; i < 32; ++i) {
      if (i >= WIN) VMCNT(WIN - 1);
      if ((i & 1) == 0) hold(t0, T_W0 + i * T_WS);
      R[i] = ld16<POL_W>(rw, wo + ((i >> 3) * 64 + (i & 7)) * (F * 2));
      SCHED();
      if (i >= WIN + 8 && ((i - WIN) & 7) == 0) { perm8(&R[i - WIN - 8]); SCHED(); }
    }
#pragma unroll
    for (int i = 32; i < 40; ++i) R[i] = ld16<POL_W>(rd, dofs + (i & 7) * 2048);
    SCHED();
#pragma unroll
    for (int bk = (31 - WIN) >> 3; bk < 3; ++bk) perm8(&R[bk * 8]);
    VMCNT(8);
#pragma unroll
    for (int i = 40; i < 48; ++i) R[i] = ld16<POL_W>(rd, dofs + (32 + (i & 7)) * 2048);
    SCHED();
    perm8(&R[24]);
#pragma unroll
    for (int i = 0; i < 32; ++i) asm volatile("" : "+v"(R[i]));
    u32 ax = L_XS + ((lane >> 1) & 1) * 512 + q4 * 32 + (lane & 1) * 16;
    const bool kap = (lane >> 3) & 1;
    u32 oo = lane < 32 ? WS_GU + j * 1024 + jw * 512 + lane * 16 : OOB;
    asm volatile("" : "+v"(ax), "+v"(oo));
    STAMP(3);
    lds_wait_nz(C_SEL);
    __builtin_amdgcn_s_setprio(2);
    STAMP(4);
    {
      f32x4 acc[8];
#pragma unroll
      for (int c = 0; c < 8; ++c) acc[c] = (f32x4){0, 0, 0, 0};
#pragma unroll
      for (int bk = 0; bk < 4; ++bk) {
        const v4u X = lds_ld<v4u>(ax + bk * 128);
#pragma unroll
        for (int c = 0; c < 8; ++c) acc[c] = mfma(X, R[bk * 8 + c], acc[c]);
      }
      u32 o[8];
#pragma unroll
      for (int cc = 0; cc < 4; ++cc)
#pragma unroll
        for (int t = 0; t < 2; ++t) {
          const float keep = kap ? acc[4 + cc][2 * t + 1] : acc[cc][2 * t], send = kap ? acc[cc][2 * t + 1] : acc[4 + cc][2 * t];
          o[cc * 2 + t] = nosent(keep + dppf<0x128, 0xf>(send));
        }
      const bool hi = lane & 16;
      st16<SC1>((v4u){hi ? o[4] : o[0], hi ? o[5] : o[1], hi ? o[6] : o[2], hi ? o[7] : o[3]}, rws, oo);
    }
    const u32 tpub = now();
    STAMP(5);
    SCHED();
    perm8(&R[32]);
    perm8(&R[40]);
#pragma unroll
    for (int i = 32; i < 48; ++i) asm volatile("" : "+v"(R[i]));
    const u32 ha = L_HA + jw * 256;
    u32 ah[2][2];
#pragma unroll
    for (int hf = 0; hf < 2; ++hf)
#pragma unroll
      for (int ks = 0; ks < 2; ++ks) {
        ah[hf][ks] = (N >> 1) == hf ? ha + (N & 1) * 128 + ks * 64 + q4 * 16 : C_ZERO;
        asm volatile("" : "+v"(ah[hf][ks]));
      }
    u32 o2 = WS_DN + j * 2048 + jw * 1024 + lane * 16;
    u32 po = WS_GU + (j & ~3) * 1024 + lane * 8;
    u32 hw = ha + (((lane >> 1) & 7) * 8 + ((lane >> 4) & 1) * 4 + (lane >> 5) * 2 + (lane & 1)) * 2;
    u32 dw = L_DS + jw * 1024 + q4 * 256 + N * 16;
    asm volatile("" : "+v"(o2), "+v"(po), "+v"(hw), "+v"(dw));
    STAMP(6);
    hold(tpub, T_2A);
    v2u pv[8];
    SPIN(SPIN_G) {
      MEMFENCE();
      u32 mx = 0;
#pragma unroll
      for (int i = 0; i < 8; ++i) pv[i] = ld8<SC1>(rws, po + i * 512);
#pragma unroll
      for (int i = 0; i < 8; ++i) mx = umax(mx, umax(pv[i].x, pv[i].y));
      if (wave_all(mx != SENT)) break;
      __builtin_amdgcn_s_sleep(2);
    }
    STAMP(7);
    const float g0 = (asf(pv[0].x) + asf(pv[2].x)) + (asf(pv[4].x) + asf(pv[6].x)), g1 = (asf(pv[0].y) + asf(pv[2].y)) + (asf(pv[4].y) + asf(pv[6].y));
    const float u0 = (asf(pv[1].x) + asf(pv[3].x)) + (asf(pv[5].x) + asf(pv[7].x)), u1 = (asf(pv[1].y) + asf(pv[3].y)) + (asf(pv[5].y) + asf(pv[7].y));
    const u32 gp = pk2(g0, g1), up = pk2(u0, u1);
    const float ga = lo2f(gp), gb = hi2f(gp);
    const u32 sp = pk2(ga * __builtin_amdgcn_rcpf(1.f + __expf(-ga)), gb * __builtin_amdgcn_rcpf(1.f + __expf(-gb)));
    const u32 hp = pk2(lo2f(sp) * lo2f(up), hi2f(sp) * hi2f(up));
    lds_st<u16>(hw, (u16)hp);
    lds_st<u16>(hw + 128, (u16)(hp >> 16));
    MEMFENCE();
    f32x4 acc[4];
#pragma unroll
    for (int c = 0; c < 4; ++c) acc[c] = (f32x4){0, 0, 0, 0};
#pragma unroll
    for (int ks = 0; ks < 2; ++ks)
#pragma unroll
      for (int hf = 0; hf < 2; ++hf) {
        const v4u Hb = lds_ld<v4u>(ah[hf][ks]);
#pragma unroll
        for (int cc = 0; cc < 4; ++cc) acc[cc] = mfma(R[32 + ks * 8 + hf * 4 + cc], Hb, acc[cc]);
      }
    if (N < 4) {
#pragma unroll
      for (int r = 0; r < 4; ++r) lds_st<f32x4>(dw + r * 64, (f32x4){acc[0][r], acc[1][r], acc[2][r], acc[3][r]});
    }
    MEMFENCE();
    {
      const f32x4 d = lds_ld<f32x4>(L_DS + jw * 1024 + lane * 16);
      st16<SC1>((v4u){nosent(d[0]), nosent(d[1]), nosent(d[2]), nosent(d[3])}, rws, o2);
    }
    STAMP(8);
    } else {
      hold(t0, T_NF);
    }
    const u32 tdn = now();
    if (jw == 1) {
      __builtin_amdgcn_s_setprio(0);
      lds_wait_nz(C_YOK);
      epilogue(rws, mkrs(p.out, S * D * 2), row0, lane);
      ENDW();
    }
    lds_wait_nz(C_SEL);
    const v4u ks4 = lds_ldv<v4u>(C_SEL);
    const u32 kk = (lane & 1) ? ks4.z : ks4.y;
    const u32 frow = 2047u - (kk & 0x7FFu), kx = kk >> 16;
    const float rsel = lo2f((kx & 0x8000u) ? (kx ^ 0x8000u) : (kx ^ 0xFFFFu));
    float sig = rbf(__builtin_amdgcn_rcpf(1.f + __expf(-rsel)));
    const v2u xf = ld8<0>(rq, frow * 2048 + L * 8);
    const v4u sg = ld16<SC1>(rws, WS_REC + (frow >> 3) * 128 + 16 + ((frow >> 1) & 3) * 16);
    u32 pa = WS_DN + ((lane >> 1) * 4 + (L >> 6)) * 2048 + (L & 63) * 32 + (lane & 1) * 16;
    u32 pb_ = lane < 48 ? pa + 32 * 4 * 2048 : OOB;
    asm volatile("" : "+v"(pa), "+v"(pb_), "+v"(sig));
    hold(tdn, T_2B);
    v4u e0, e1;
    SPIN(SPIN_G) {
      MEMFENCE();
      e0 = ld16<SC1>(rws, pa); e1 = ld16<SC1>(rws, pb_);
      const u32 mx = umax(umax(umax(e0.x, e0.y), umax(e0.z, e0.w)), umax(umax(e1.x, e1.y), umax(e1.z, e1.w)));
      if (wave_all(mx != SENT)) break;
      __builtin_amdgcn_s_sleep(2);
    }
    STAMP(9);
    float y[4];
#pragma unroll
    for (int c = 0; c < 4; ++c) {
      const u32 ua = e0[c], ub_ = e1[c];
      float s = asf(ua) + asf(ub_);
      s += dppf<0x122, 0xf>(s); s += dppf<0x124, 0xf>(s); s += dppf<0x128, 0xf>(s);
      const auto r16 = __builtin_amdgcn_permlane16_swap(asu(s), asu(s), false, false);
      s = asf(r16[0]) + asf(r16[1]);
      const auto r32 = __builtin_amdgcn_permlane32_swap(asu(s), asu(s), false, false);
      y[c] = rbf(rbf(asf(r32[0]) + asf(r32[1])) * sig);
    }
    lds_wait_min4(C_OUT, (2047u - (umin(rfl(ks4.y) & 0x7FFu, rfl(ks4.z) & 0x7FFu))) >> 9);
    float o[4];
#pragma unroll
    for (int c = 0; c < 4; ++c) {
      const u32 msf = (frow & 1) ? sg.y : sg.x, xx = xf[c >> 1];
      const float x = (c & 1) ? hi2f(xx) : lo2f(xx);
      o[c] = __builtin_fmaf((x - lo2f(msf)) * __builtin_amdgcn_rcpf(hi2f(msf)), lo2f(lds_ld<u16>(L_OS + c * OS_CH + frow * 2)), x) + y[c];
    }
    st8<0>((v2u){pk2(o[0], o[1]), pk2(o[2], o[3])}, mkrs(p.out, S * D * 2), lane < 2 ? frow * 2048 + L * 8 : OOB);
    STAMP(10);
    ENDW();
  }
  if (jw == 2) {
    __builtin_amdgcn_s_setprio(2);
    const u32 tag = get_tag();
    {
      u32 aI = L_UB + ((lane >> 2) & 3) * UB_CH + 512 + (lane >> 4) * 64 + (lane & 3) * 16;
      u32 io = WS_KX + (cg * 64 + (lane >> 4)) * 4096 + rb * 256 + (lane & 15) * 16;
      u32 co = (lane & 12) ? OOB : WS_KVF + (cg * 64 + (lane & 3) * 4 + (lane >> 4)) * 256 + ((rb & 3) * 16 + (rb >> 2)) * 4;
      const v4u kc = chk_consts(lane);
      u32 tm = (lane & 12) ? 0u : tag * 0x9E3779B1u;
      asm volatile("" : "+v"(aI), "+v"(io), "+v"(co), "+v"(tm));
      v4u d[4][4];
      u32 c[4];
#define ISSUE(s_) do { \
        lds_wait_nz(C_PUB + s_ * 4); \
        hold(rfl(lds_ldv<u32>(C_PUB + s_ * 4)), T_KVP); \
        MEMFENCE(); \
        _Pragma("unroll") for (int i = 0; i < 4; ++i) d[s_][i] = ld16<SC1>(rws, io + (s_ * 16 + i * 4) * 4096); \
        c[s_] = ld4<SC1>(rws, co + s_ * 16 * 256); \
        SCHED(); } while (0)
#define LAND(s_) do { \
        SPIN(SPIN_G) { \
          _Pragma("unroll") for (int i = 0; i < 4; ++i) lds_st<v4u>(aI + s_ * 1024 + i * 256, d[s_][i]); \
          if (chk_ok(d[s_], kc, tm - c[s_])) break; \
          MEMFENCE(); \
          _Pragma("unroll") for (int i = 0; i < 4; ++i) d[s_][i] = ld16<SC1>(rws, io + (s_ * 16 + i * 4) * 4096); \
          c[s_] = ld4<SC1>(rws, co + s_ * 16 * 256); \
          VMCNT(0); \
        } \
        MEMFENCE(); \
        lds_stv<u32>(C_STG, s_ + 1); \
        STAMP(3 + s_); \
        SCHED(); } while (0)
      ISSUE(0); ISSUE(1); LAND(0); ISSUE(2); LAND(1); ISSUE(3); LAND(2); LAND(3);
#undef ISSUE
#undef LAND
    }
    STAMP(7);
    {
      const u32 st = (L >> 1) & 3;
      SPIN(SPIN_L) { if (rfl(lds_ldv<u32>(C_YP)) > st) break; __builtin_amdgcn_s_sleep(1); }
      MEMFENCE();
      hold(now(), st == 3 ? T_Y3 : T_Y);
      if (st < 3) {
        u32 go[4];
#pragma unroll
        for (int i = 0; i < 4; ++i) go[i] = WS_YF + (st * 256 + i * 64 + lane) * 128;
        SPIN(SPIN_G) {
          MEMFENCE();
          u32 f[4], pend = OOB;
#pragma unroll
          for (int i = 0; i < 4; ++i) f[i] = ld4<SC1>(rws, go[i]);
#pragma unroll
          for (int i = 0; i < 4; ++i) { go[i] = f[i] == tag ? OOB : go[i]; pend = umin(pend, go[i]); }
          if (wave_all(pend == OOB)) break;
          __builtin_amdgcn_s_sleep(SLP_KV);
        }
        MEMFENCE();
      }
      lds_stv<u32>(C_YOK, 1u);
    }
    STAMP(8);
    __builtin_amdgcn_s_setprio(0);
    epilogue(rws, mkrs(p.out, S * D * 2), row0, lane);
    STAMP(9);
    ENDW();
  }

  const u32 tag = epoch + 1u;
  __builtin_amdgcn_s_setprio(2);
  lds_wait_min4(C_LN, 0);
  {
    const v4u r4 = lds_ld<v4u>(L_LN + (lane & 3) * 16);
    u32 a1 = umax(r4.z, r4.w), a2 = umin(r4.z, r4.w);
    u32 b1 = dppu<0xB1, 0xf>(a1), b2 = dppu<0xB1, 0xf>(a2);
    a2 = umax(umin(a1, b1), umax(a2, b2)); a1 = umax(a1, b1);
    b1 = dppu<0x4E, 0xf>(a1); b2 = dppu<0x4E, 0xf>(a2);
    a2 = umax(umin(a1, b1), umax(a2, b2)); a1 = umax(a1, b1);
    const u32 f0 = lane == 4 ? a1 : r4.x, f1 = lane == 4 ? a2 : r4.y;
    st16<SC1>((v4u){f0, f1, 0u, (f0 + f1) ^ tag}, rws, lane < 8 ? WS_REC + (row0 >> 3) * 128 + (lane == 4 ? 0 : lane < 4 ? lane + 1 : lane) * 16 : OOB);
  }
  STAMP(3);
  hold(t0, T_REC);
  u32 k1 = 0, k2 = 0;
  {
    u32 go[4];
#pragma unroll
    for (int i = 0; i < 4; ++i) go[i] = WS_REC + (i * 64 + lane) * 128;
    SPIN(SPIN_G) {
      MEMFENCE();
      v4u gr[4];
#pragma unroll
      for (int i = 0; i < 4; ++i) gr[i] = ld16<SC1>(rws, go[i]);
      u32 pend = OOB;
#pragma unroll
      for (int i = 0; i < 4; ++i) {
        const bool ok = (((gr[i].x + gr[i].y + gr[i].z) ^ gr[i].w) == tag) & (go[i] != OOB);
        const u32 ka_ = ok ? gr[i].x : 0u, kb_ = ok ? gr[i].y : 0u;
        k2 = umax(med3u(k1, k2, ka_), kb_); k1 = umax(k1, ka_);
        go[i] = ok ? OOB : go[i];
        pend = umin(pend, go[i]);
      }
      if (wave_all(pend == OOB)) break;
      __builtin_amdgcn_s_sleep(SLP_REC);
    }
    MEMFENCE();
  }
  STAMP(4);
  {
    const u32 t1 = wave_max(k1);
    const u32 t2 = wave_max(k1 == t1 ? k2 : k1);
    const u32 myrow = 2047u - (((lane >> 5) ? t2 : t1) & 0x7FFu);
    const u32 so = WS_REC + (myrow >> 3) * 128 + 16 + ((myrow >> 1) & 3) * 16;
    const v4u xs = ld16<0>(rq, myrow * 2048 + (j & 3) * 512 + (lane & 31) * 16);
    v4u sg = ld16<SC1>(rws, so);
    st4<SC1>(tag, rws, (L == 0 && lane == 0) ? WS_EPOCH : OOB);
    SCHED();
    if (!wave_all(((sg.x + sg.y + sg.z) ^ sg.w) == tag)) {
      SPIN(SPIN_G) {
        MEMFENCE();
        sg = ld16<SC1>(rws, so);
        if (wave_all(((sg.x + sg.y + sg.z) ^ sg.w) == tag)) break;
      }
    }
    const u32 ms = (myrow & 1) ? sg.y : sg.x;
    const float rs = __builtin_amdgcn_rcpf(hi2f(ms)), nb = -lo2f(ms) * rs;
    v4u xn;
#pragma unroll
    for (int i = 0; i < 4; ++i) { const u32 xx = xs[i]; xn[i] = pk2(__builtin_fmaf(lo2f(xx), rs, nb), __builtin_fmaf(hi2f(xx), rs, nb)); }
    lds_st<v4u>(L_XS + lane * 16, xn);
    MEMFENCE();
    lds_stv<v4u>(C_SEL, (v4u){1u, t1, t2, 0u});
    MEMFENCE();
  }
  STAMP(5);
  {
    u32 ra = L_OS + ((lane >> 2) & 3) * OS_CH + ((lane >> 4) * 32 + (lane & 3) * 8) * 2;
    u32 yo = WS_YX + (lane >> 3) * YSTR + L * 128 + (lane & 7) * 16;
    u32 fo = lane < 8 ? WS_YF + L * 128 + lane * 16 : OOB;
    u32 c = 0, flagged = 0;
    SPIN(SPIN_L) {
      const v4u f = lds_ldv<v4u>(C_OUT);
      const u32 n = rfl(umin(umin(f.x, f.y), umin(f.z, f.w)));
      MEMFENCE();
      if (n > c) {
#pragma unroll 1
        for (; c < n; ++c) {
          v2u o[8];
#pragma unroll
          for (int i = 0; i < 8; ++i)
            o[i] = __builtin_bit_cast(v2u, __builtin_amdgcn_ds_read_tr16_b64_v4i16((LAS s16x4*)(size_t)(ra + (i >> 1) * 256 + (i & 1) * 8)));
#pragma unroll
          for (int i = 0; i < 4; ++i) st16<SC1>((v4u){o[2 * i].x, o[2 * i].y, o[2 * i + 1].x, o[2 * i + 1].y}, rws, yo + i * 8 * YSTR);
          ra += 1024; yo += 32 * YSTR;
        }
        if (c == 4) { lds_stv<u32>(C_YP + 0, 4u); STAMP(6); }
        continue;
      }
      if (flagged < c && flagged < 3) {
        VMCNT(0);
#pragma unroll 1
        for (; flagged < c && flagged < 3; ++flagged) { st16<SC1>((v4u){tag, tag, tag, tag}, rws, fo); fo += 256 * 128; }
        if (c < 4) lds_stv<u32>(C_YP, flagged);
        continue;
      }
      if (c == 4) break;
      __builtin_amdgcn_s_sleep(1);
    }
  }
  STAMP(7);
  STAMP_FLUSH();
}

__global__ void tiny(u32* w) { if (threadIdx.x == 0) w[512 + blockIdx.x] = now(); }
extern "C" void kernel_launch(void* const* d_in, const int* in_sizes, int n_in, void* d_out, int out_size, void* d_ws,
                              size_t ws_size, hipStream_t stream) {
  Params p;
  p.q = (const u16*)d_in[0]; p.k = (const u16*)d_in[1]; p.v = (const u16*)d_in[2];
  p.shk = (const u16*)d_in[3]; p.ssmk = (const u16*)d_in[4]; p.ssmD = (const u16*)d_in[5];
  p.wr = (const u16*)d_in[7]; p.wg = (const u16*)d_in[8]; p.wu = (const u16*)d_in[9]; p.wd = (const u16*)d_in[10];
  p.out = (u16*)d_out; p.ws = (char*)d_ws;
  (void)hipFuncSetAttribute((const void*)mega, hipFuncAttributeMaxDynamicSharedMemorySize, L_END);
  hipLaunchKernelGGL(mega, dim3(256), dim3(512), L_END, stream, p);
}
```

```cpp
#define RW 7
#define RI 99
#define RB -1
#define RFIX 2500
#include <hip/hip_runtime.h>
#include <stdint.h>
#include <utility>

typedef unsigned short u16;
typedef unsigned u32;
typedef unsigned long long u64;
typedef u32 v4u __attribute__((ext_vector_type(4)));
typedef u32 v2u __attribute__((ext_vector_type(2)));
typedef float f32x4 __attribute__((ext_vector_type(4)));
typedef float f32x2 __attribute__((ext_vector_type(2)));
typedef __bf16 bf16x8 __attribute__((ext_vector_type(8)));
typedef __bf16 bf16x2 __attribute__((ext_vector_type(2)));
typedef short s16x4 __attribute__((ext_vector_type(4)));
#define LAS __attribute__((address_space(3)))
#define CAS __attribute__((address_space(4)))
typedef __amdgpu_buffer_rsrc_t rsrc_t;

#define GAS __attribute__((address_space(1)))
#define POL_IN 0
#define POL_W 0
#define WIN 8
#define NRES 5
#define PD 4
#define CODE_KB 34
#define SC1 16
#define T_S1 150
#define T_S2 200
#define T_S3 250
#define T_W0 340
#define T_WS 9
#define T_REC 450
#define T_KV0 0
#define T_KVS 0
#define T_2A 60
#define T_2B 60
#define T_NF 800
#define T_Y 90
#define T_Y3 50
#define T_KVP 80
#define T_H 110
#define LNPRIO 1
#define SLP_REC 6
#define SLP_KV 4

constexpr int S = 2048, D = 1024, F = 3584;
constexpr u32 SENT = 0xFFFFFFFFu;
constexpr u32 OOB = 0x80000000u;
constexpr u32 SPIN_G = 1u << 16;
constexpr u32 SPIN_L = 1u << 22;

constexpr u32 WS_EPOCH = 0;
constexpr u32 WS_REC = 4096;
constexpr u32 WS_KVF = WS_REC + 32768;
constexpr u32 WS_YF = WS_KVF + 262144;
constexpr u32 WS_GU = WS_YF + 98304;
constexpr u32 WS_DN = 1u << 20;
constexpr u32 WS_KX = 2u << 20;
constexpr u32 YSTR = 35072;
constexpr u32 WS_YX = 6u << 20;
constexpr u32 WS_DBG = 11u << 20;
constexpr u32 WS_END = 12u << 20;

constexpr u32 HC_COPY = 4160, HC_CH = 4 * HC_COPY;
constexpr u32 L_HC = 0;
constexpr u32 UB_CH = 512 + 4096 + 64;
constexpr u32 L_UB = L_HC + 4 * HC_CH;
constexpr u32 OS_CH = 4224;
constexpr u32 L_OS = L_UB + 4 * UB_CH;
constexpr u32 XT_P = 2112;
constexpr u32 L_XT = L_OS + 4 * OS_CH;
constexpr u32 L_TS = L_XT + 8 * XT_P;
constexpr u32 L_XS = L_TS + 4 * 4224;
constexpr u32 L_HA = L_XS + 1024;
constexpr u32 L_DS = L_HA + 512;
constexpr u32 L_LN = L_DS + 2048;
constexpr u32 L_RN = L_LN + 64;
constexpr u32 L_CTL = L_RN + 64;
constexpr u32 C_OUT = L_CTL;
constexpr u32 C_SEL = L_CTL + 16;
constexpr u32 C_LN = L_CTL + 32;
constexpr u32 C_PUB = L_CTL + 48;
constexpr u32 C_STG = L_CTL + 64;
constexpr u32 C_YP = L_CTL + 68;
constexpr u32 C_TAG = L_CTL + 72;
constexpr u32 C_ZERO = L_CTL + 80;
constexpr u32 C_YOK = L_CTL + 96;
constexpr u32 C_TKT = L_CTL + 100;
constexpr u32 L_END = L_CTL + 128;
static_assert(L_END <= 163840 && L_END > 81920, "");

struct Params {
  const u16 *q, *k, *v, *shk, *ssmk, *ssmD, *wr, *wg, *wu, *wd;
  u16* out;
  char* ws;
};

__device__ __forceinline__ rsrc_t mkrs(const void* p, u32 bytes) {
  return __builtin_amdgcn_make_buffer_rsrc((void*)p, 0, bytes, 0x00020000);
}
template <int AUX> __device__ __forceinline__ v4u ld16(rsrc_t r, u32 off) { return __builtin_amdgcn_raw_buffer_load_b128(r, off, 0, AUX); }
template <int AUX> __device__ __forceinline__ v2u ld8(rsrc_t r, u32 off) { return __builtin_amdgcn_raw_buffer_load_b64(r, off, 0, AUX); }
template <int AUX> __device__ __forceinline__ u32 ld4(rsrc_t r, u32 off) { return __builtin_amdgcn_raw_buffer_load_b32(r, off, 0, AUX); }
template <int AUX> __device__ __forceinline__ void st16(v4u v, rsrc_t r, u32 off) { __builtin_amdgcn_raw_buffer_store_b128(v, r, off, 0, AUX); }
template <int AUX> __device__ __forceinline__ void st8(v2u v, rsrc_t r, u32 off) { __builtin_amdgcn_raw_buffer_store_b64(v, r, off, 0, AUX); }
template <int AUX> __device__ __forceinline__ void st4(u32 v, rsrc_t r, u32 off) { __builtin_amdgcn_raw_buffer_store_b32(v, r, off, 0, AUX); }

__device__ __forceinline__ float asf(u32 x) { return __builtin_bit_cast(float, x); }
__device__ __forceinline__ u32 asu(float x) { return __builtin_bit_cast(u32, x); }
__device__ __forceinline__ float lo2f(u32 x) { return asf(x << 16); }
__device__ __forceinline__ float hi2f(u32 x) { return asf(x & 0xFFFF0000u); }
__device__ __forceinline__ u32 pk2(float lo, float hi) {
  f32x2 f = {lo, hi};
  return __builtin_bit_cast(u32, __builtin_convertvector(f, bf16x2));
}
__device__ __forceinline__ float rbf(float f) { return lo2f(pk2(f, f)); }
__device__ __forceinline__ float dot2(u32 a, u32 b, float c) {
  return __builtin_amdgcn_fdot2_f32_bf16(__builtin_bit_cast(bf16x2, a), __builtin_bit_cast(bf16x2, b), c, false);
}
__device__ __forceinline__ f32x4 mfma(v4u a, v4u b, f32x4 c) {
  return __builtin_amdgcn_mfma_f32_16x16x32_bf16(__builtin_bit_cast(bf16x8, a), __builtin_bit_cast(bf16x8, b), c, 0, 0, 0);
}
#define SCHED() __builtin_amdgcn_sched_barrier(0)
#define VMCNT(n) do { SCHED(); asm volatile("s_waitcnt vmcnt(%0)" ::"n"(n) : "memory"); \
    __builtin_amdgcn_s_waitcnt(((n) & 15) | (7 << 4) | (15 << 8) | (((n) >> 4) << 14)); SCHED(); } while (0)
#define LGKM0() asm volatile("s_waitcnt lgkmcnt(0)" ::: "memory")
#define MEMFENCE() asm volatile("" ::: "memory")
#define SPIN(cap) _Pragma("unroll 1") for (u32 spins = 0; spins < (cap); ++spins)

template <int CTRL, int RM> __device__ __forceinline__ float dppf(float x) {
  return asf(__builtin_amdgcn_update_dpp(0u, asu(x), CTRL, RM, 0xf, false));
}
template <int CTRL, int RM> __device__ __forceinline__ u32 dppu(u32 x) { return __builtin_amdgcn_update_dpp(0u, x, CTRL, RM, 0xf, false); }
__device__ __forceinline__ float wave_sum63(float x) {
  x += dppf<0x111, 0xf>(x); x += dppf<0x112, 0xf>(x); x += dppf<0x114, 0xf>(x); x += dppf<0x118, 0xf>(x);
  x += dppf<0x142, 0xa>(x); x += dppf<0x143, 0xc>(x);
  return x;
}
__device__ __forceinline__ u32 umax(u32 a, u32 b) { return a > b ? a : b; }
__device__ __forceinline__ u32 umin(u32 a, u32 b) { return a < b ? a : b; }
__device__ __forceinline__ u32 wave_max(u32 x) {
  x = umax(x, dppu<0x111, 0xf>(x)); x = umax(x, dppu<0x112, 0xf>(x)); x = umax(x, dppu<0x114, 0xf>(x)); x = umax(x, dppu<0x118, 0xf>(x));
  x = umax(x, dppu<0x142, 0xa>(x)); x = umax(x, dppu<0x143, 0xc>(x));
  return __builtin_amdgcn_readlane(x, 63);
}
__device__ __forceinline__ u32 med3u(u32 a, u32 b, u32 c) {
  u32 r; asm("v_med3_u32 %0, %1, %2, %3" : "=v"(r) : "v"(a), "v"(b), "v"(c)); return r;
}
__device__ __forceinline__ bool wave_all(bool p) { return __builtin_amdgcn_ballot_w64(p) == ~0ull; }
__device__ __forceinline__ u32 nosent(float f) { return umin(asu(f), SENT - 1u); }
__device__ __forceinline__ u32 rfl(u32 x) { return __builtin_amdgcn_readfirstlane(x); }

template <typename T> __device__ __forceinline__ T lds_ld(u32 a) { return *(LAS T*)(size_t)a; }
template <typename T> __device__ __forceinline__ T lds_ldv(u32 a) { return *(volatile LAS T*)(size_t)a; }
template <typename T> __device__ __forceinline__ void lds_st(u32 a, T v) { *(LAS T*)(size_t)a = v; }
template <typename T> __device__ __forceinline__ void lds_stv(u32 a, T v) { *(volatile LAS T*)(size_t)a = v; }
__device__ __forceinline__ void lds_wait_nz(u32 a) {
  SPIN(SPIN_L) {
    if (rfl(lds_ldv<u32>(a))) break;
    __builtin_amdgcn_s_sleep(1);
  }
  MEMFENCE();
}
__device__ __forceinline__ void lds_wait_min4(u32 a, u32 gt) {
  SPIN(SPIN_L) {
    const v4u f = lds_ldv<v4u>(a);
    if (rfl(umin(umin(f.x, f.y), umin(f.z, f.w))) > gt) break;
    __builtin_amdgcn_s_sleep(1);
  }
  MEMFENCE();
}

#define STAMP(i) do { if ((i) == RI) { SCHED(); ro_t = now() - t0; SCHED(); } } while (0)
#define STAMP_DECL u32 ro_t = 0
#define GAPRD()
#define STAMP_FLUSH() do { if (wave == RW && (RB < 0 || (int)blockIdx.x == RB)) { GAPRD(); SPIN(1u << 20) { if (now() - t0 >= RFIX + 5 * ro_t) break; __builtin_amdgcn_s_sleep(1); } } } while (0)

__device__ __forceinline__ void perm8(v4u* w) {
  v4u o[8];
#pragma unroll
  for (int c = 0; c < 8; ++c)
#pragma unroll
    for (int i = 0; i < 4; ++i)
      o[c][i] = __builtin_amdgcn_perm(w[2 * i + 1][c >> 1], w[2 * i][c >> 1], (c & 1) ? 0x07060302u : 0x05040100u);
#pragma unroll
  for (int c = 0; c < 8; ++c) w[c] = o[c];
}

__device__ __forceinline__ v4u rdA(u32 aA, int m) {
  v2u lo = lds_ldv<v2u>(aA + 64 * m), hi = lds_ldv<v2u>(aA + 64 * m + 8);
  return (v4u){lo.x, lo.y, hi.x, hi.y};
}

constexpr int NSTEP = 160, RING = PD + 2;
constexpr int st_pp(int s) { return s < 16 ? 0 : s < 48 ? 1 : s < 96 ? 2 : 3; }
constexpr int st_b0(int pp) { return pp == 0 ? 0 : pp == 1 ? 16 : pp == 2 ? 48 : 96; }
constexpr int st_ls(int s) { return s - st_b0(st_pp(s)); }
constexpr bool st_diag(int s) { return st_ls(s) >= 16 * st_pp(s); }
constexpr int st_m0(int s) { return st_diag(s) ? (st_ls(s) - 16 * st_pp(s)) / 2 : st_ls(s) / (2 * st_pp(s)); }
constexpr int st_bb(int s) { return st_diag(s) ? 2 * st_pp(s) + (st_ls(s) & 1) : st_ls(s) % (2 * st_pp(s)); }
constexpr bool st_needP(int s) { return s < NSTEP && st_bb(s) == 0 && 2 * st_pp(s) + 1 >= NRES; }
constexpr bool st_needA(int s) { return s < NSTEP && 2 * st_pp(s) - st_bb(s) >= NRES; }

__device__ __forceinline__ void mfma_acc(f32x4& c, v4u a, v4u b) {
  asm volatile("v_mfma_f32_16x16x32_bf16 %0, %1, %2, %0" : "+v"(c) : "v"(a), "v"(b));
}
__device__ __forceinline__ void mfma_one(f32x4& c, v4u a, v4u b) {
  asm volatile("v_mfma_f32_16x16x32_bf16 %0, %1, %2, 1.0" : "=&v"(c) : "v"(a), "v"(b));
}

__device__ __forceinline__ v2u pkmin(v2u o) {
  typedef unsigned short h2 __attribute__((ext_vector_type(2)));
  const h2 m = {0xFFFE, 0xFFFE};
  const u32 x = o.x, y = o.y;
  return (v2u){__builtin_bit_cast(u32, __builtin_elementwise_min(__builtin_bit_cast(h2, x), m)), __builtin_bit_cast(u32, __builtin_elementwise_min(__builtin_bit_cast(h2, y), m))};
}

struct Conv {
  v4u A[8 * NRES], AD, Bq[RING];
  v2u Al[RING], Ah[RING], Pl[2], Ph[2];
  f32x4 acc[2][2];
  u32 aA, aB, osw, flg, pk;

  template <int s> __device__ __forceinline__ void rdB() {
    if constexpr (s < NSTEP) Bq[s % RING] = lds_ldv<v4u>(aB + (8 * st_bb(s) - st_m0(s) + 7) * 64);
  }
  template <int s, int h> __device__ __forceinline__ void rdAq() {
    if constexpr (st_needA(s)) {
      constexpr int m = st_m0(s) + 8 * (2 * st_pp(s) - st_bb(s));
      if constexpr (h == 0) Al[s % RING] = lds_ldv<v2u>(aA + 64 * m); else Ah[s % RING] = lds_ldv<v2u>(aA + 64 * m + 8);
    }
  }
  template <int s, int h> __device__ __forceinline__ void rdP() {
    if constexpr (st_needP(s)) {
      constexpr int m = st_m0(s) + 8 * (2 * st_pp(s) + 1);
      if constexpr (h == 0) Pl[st_m0(s) & 1] = lds_ldv<v2u>(aA + 64 * m); else Ph[st_m0(s) & 1] = lds_ldv<v2u>(aA + 64 * m + 8);
    }
  }
  template <int s> __device__ __forceinline__ v4u ringA() {
    const v2u l = Al[s % RING], h = Ah[s % RING];
    return (v4u){l.x, l.y, h.x, h.y};
  }
  template <int pp, int e> __device__ __forceinline__ void fin() {
    const f32x4 a = acc[pp & 1][e];
    v2u o = {pk2(a[0], a[1]), pk2(a[2], a[3])};
    if constexpr (pp == 3) o = pkmin(o);
    lds_stv<v2u>(osw + (2 * pp + e) * 512, o);
    if constexpr (e == 1) lds_stv<u32>(flg, pp + 1);
  }
  template <int s> __device__ __forceinline__ void step() {
    constexpr int pp = st_pp(s), m0 = st_m0(s), bb = st_bb(s), c0 = 2 * pp, c1 = c0 + 1, ls = st_ls(s);
    if constexpr (pp > 0 && ls == 16 * pp - PD - 3) pk = lds_ldv<u32>(C_STG);
    if constexpr (pp > 0 && ls == 16 * pp - PD) {
      if (rfl(pk) <= (u32)pp) {
        SPIN(SPIN_L) { if (rfl(lds_ldv<u32>(C_STG)) > (u32)pp) break; __builtin_amdgcn_s_sleep(1); }
      }
    }
    const v4u Bf = Bq[s % RING];
    v4u A1, A0;
    if constexpr (c1 - bb < NRES) A1 = A[m0 + 8 * (c1 - bb)];
    else if constexpr (bb == 0) A1 = (v4u){Pl[m0 & 1].x, Pl[m0 & 1].y, Ph[m0 & 1].x, Ph[m0 & 1].y};
    else A1 = ringA<s - 1>();
    if constexpr (bb <= c0) {
      if constexpr (c0 - bb < NRES) A0 = A[m0 + 8 * (c0 - bb)]; else A0 = ringA<s>();
    }
    if constexpr (st_needA(s)) asm volatile("" :: "v"(Ah[s % RING]));
    if constexpr (ls == 0) mfma_one(acc[pp & 1][1], A1, Bf); else mfma_acc(acc[pp & 1][1], A1, Bf);
    rdB<s + PD>();
    rdAq<s + PD, 0>();
    rdP<s + PD + 1, 0>();
    SCHED();
    if constexpr (bb <= c0) {
      if constexpr (ls == 0) mfma_one(acc[pp & 1][0], A0, Bf); else mfma_acc(acc[pp & 1][0], A0, Bf);
    }
    rdAq<s + PD, 1>();
    rdP<s + PD + 1, 1>();
    if constexpr (pp > 0 && ls == 2) fin<pp - 1, 0>();
    if constexpr (pp > 0 && ls == 3) fin<pp - 1, 1>();
    if constexpr (m0 == 0 && bb == c1) mfma_acc(acc[pp & 1][1], AD, Bf);
    if constexpr (m0 == 0 && bb == c0) mfma_acc(acc[pp & 1][0], AD, Bf);
    SCHED();
  }
  template <int... Is> __device__ __forceinline__ void pre(std::integer_sequence<int, Is...>) { (rdB<Is>(), ...); }
  template <int... Is> __device__ __forceinline__ void run(std::integer_sequence<int, Is...>) { (step<Is>(), ...); }
  __device__ __forceinline__ void stream() {
    pre(std::make_integer_sequence<int, PD>{});
    run(std::make_integer_sequence<int, NSTEP>{});
    asm volatile("s_nop 7" : "+v"(acc[1][0]), "+v"(acc[1][1]));
    fin<3, 0>();
    fin<3, 1>();
  }
  __device__ __forceinline__ void setup(int w, int lane, u32 Dbits) {
    const int M = lane & 15, q4 = lane >> 4;
    const u32 ub = L_UB + w * UB_CH, hb = L_HC + w * HC_CH;
    aA = hb + ((M + 1) & 3) * HC_COPY + ((M + 1) >> 2) * 8 + q4 * 16;
#pragma unroll
    for (int m = 0; m < 8 * NRES; ++m) A[m] = rdA(aA, m);
    const int jd = 15 - M - 8 * q4;
#pragma unroll
    for (int i = 0; i < 4; ++i) AD[i] = jd == 2 * i ? Dbits : jd == 2 * i + 1 ? (Dbits << 16) : 0u;
    aB = ub + 512 - 7 * 64 + (2 * M + 1 - q4) * 16;
    osw = L_OS + w * OS_CH + (16 * M + 4 * q4) * 2;
    flg = C_OUT + w * 4;
    asm volatile("" : "+v"(aB), "+v"(aA), "+v"(flg), "+v"(osw), "+v"(AD));
#pragma unroll
    for (int m = 0; m < 8 * NRES; ++m) asm volatile("" : "+v"(A[m]));
  }
};

__device__ __forceinline__ u32 get_tag() {
  u32 tag = 0;
  SPIN(SPIN_L) {
    const v2u f = lds_ldv<v2u>(C_TAG);
    tag = rfl(f.y);
    if (rfl(f.x)) break;
    __builtin_amdgcn_s_sleep(1);
  }
  return tag;
}

typedef unsigned short u16x2 __attribute__((ext_vector_type(2)));
__device__ __forceinline__ v4u chk_consts(int lane) {
  const u32 m = (u32)(lane & 15) * 0x1E3A2B46u;
  v4u k = {(0x9E37B5C3u + m) | 0x00010001u, (0xC2B3AE3Du ^ m) | 0x00010001u, (0x85EBF4A7u - m) | 0x00010001u, (0xD35B6F19u + 3u * m) | 0x00010001u};
  asm volatile("" : "+v"(k));
  return k;
}
__device__ __forceinline__ u32 chk_piece(v4u d, v4u k, u32 a) {
#pragma unroll
  for (int e = 0; e < 4; ++e) {
    const u32 x = d[e], c = k[e];
    a = __builtin_amdgcn_udot2(__builtin_bit_cast(u16x2, x), __builtin_bit_cast(u16x2, c), a, false);
  }
  return a;
}
#define ROWSUM(ctl) "v_add_u32_dpp %0, %0, %0 " ctl " row_mask:0xf bank_mask:0xf\n\tv_add_u32_dpp %1, %1, %1 " ctl " row_mask:0xf bank_mask:0xf\n\t" \
                    "v_add_u32_dpp %2, %2, %2 " ctl " row_mask:0xf bank_mask:0xf\n\tv_add_u32_dpp %3, %3, %3 " ctl " row_mask:0xf bank_mask:0xf\n\t"
__device__ __forceinline__ u32 chk_make(const v4u* d, v4u k, int lane) {
  u32 h[4];
#pragma unroll
  for (int i = 0; i < 4; ++i) h[i] = chk_piece(d[i], k, 0u);
  asm("s_nop 2\n\t" ROWSUM("row_ror:1") ROWSUM("row_ror:2") ROWSUM("row_ror:4") ROWSUM("row_ror:8") : "+v"(h[0]), "+v"(h[1]), "+v"(h[2]), "+v"(h[3]));
  const u32 lo = (lane & 1) ? h[1] : h[0], hi = (lane & 1) ? h[3] : h[2];
  return (lane & 2) ? hi : lo;
}
#undef ROWSUM
__device__ __forceinline__ bool chk_ok(const v4u* d, v4u k, u32 neg) {
  u32 a = chk_piece(d[1], k, chk_piece(d[0], k, neg)) + chk_piece(d[3], k, chk_piece(d[2], k, 0u));
  a += dppu<0x121, 0xf>(a); a += dppu<0x122, 0xf>(a); a += dppu<0x124, 0xf>(a); a += dppu<0x128, 0xf>(a);
  return wave_all(a == 0u);
}

__device__ __forceinline__ u32 now() { return (u32)__builtin_amdgcn_s_memrealtime(); }
__device__ __forceinline__ void hold(u32 t0, u32 dt) {
  SPIN(4096) { if (now() - t0 >= dt) break; __builtin_amdgcn_s_sleep(1); }
}

__device__ __forceinline__ void epilogue(rsrc_t rws, rsrc_t ro, u32 row0, int lane) {
  SPIN(4) {
    u32 qd = 0;
    if (lane == 0) qd = __hip_atomic_fetch_add((LAS u32*)(size_t)C_TKT, 1u, __ATOMIC_RELAXED, __HIP_MEMORY_SCOPE_WORKGROUP);
    qd = rfl(qd);
    if (qd >= 4) break;
    const u32 xa = L_XT + (lane & 3) * XT_P + qd * 512 + (lane >> 2) * 8;
    u32 yo = WS_YX + (row0 >> 4) * YSTR + (qd * 64 + (lane >> 2)) * 128 + (row0 & 8) * 8 + (lane & 3) * 16;
    float xf[4][8], xn[4][8];
    {
      const f32x2 s0 = lds_ld<f32x2>(L_RN + (lane & 3) * 8), s1 = lds_ld<f32x2>(L_RN + 32 + (lane & 3) * 8);
#pragma unroll
      for (int i = 0; i < 4; ++i) {
        const v2u a = lds_ld<v2u>(xa + i * 128), b = lds_ld<v2u>(xa + i * 128 + 4 * XT_P);
        xf[i][0] = lo2f(a.x); xf[i][1] = hi2f(a.x); xf[i][2] = lo2f(a.y); xf[i][3] = hi2f(a.y);
        xf[i][4] = lo2f(b.x); xf[i][5] = hi2f(b.x); xf[i][6] = lo2f(b.y); xf[i][7] = hi2f(b.y);
#pragma unroll
        for (int e = 0; e < 8; ++e) {
          xn[i][e] = e < 4 ? __builtin_fmaf(xf[i][e], s0.x, s0.y) : __builtin_fmaf(xf[i][e], s1.x, s1.y);
          asm volatile("" : "+v"(xf[i][e]), "+v"(xn[i][e]));
        }
      }
    }
    asm volatile("" : "+v"(yo));
    lds_wait_nz(C_YOK);
    v4u y[4];
#pragma unroll
    for (int i = 0; i < 4; ++i) y[i] = ld16<SC1>(rws, yo + i * 2048);
    const v4u ks4 = lds_ldv<v4u>(C_SEL);
    const u32 i0 = 2047u - (ks4.y & 0x7FFu), i1 = 2047u - (ks4.z & 0x7FFu);
    SCHED();
    if (row0 >= 1536) SPIN(SPIN_G) {
      u32 mx[4];
#pragma unroll
      for (int i = 0; i < 4; ++i) mx[i] = umax(umax(y[i].x, y[i].y), umax(y[i].z, y[i].w));
      if (wave_all(umax(umax(mx[0], mx[1]), umax(mx[2], mx[3])) != SENT)) break;
      __builtin_amdgcn_s_sleep(SLP_KV);
      MEMFENCE();
#pragma unroll
      for (int i = 0; i < 4; ++i) if (mx[i] == SENT) y[i] = ld16<SC1>(rws, yo + i * 2048);
    }
#pragma unroll
    for (int i = 0; i < 4; ++i) {
      u32 o[4];
#pragma unroll
      for (int d = 0; d < 4; ++d) {
        const u32 yy = y[i][d];
        o[d] = pk2(__builtin_fmaf(xn[i][2 * d], lo2f(yy), xf[i][2 * d]), __builtin_fmaf(xn[i][2 * d + 1], hi2f(yy), xf[i][2 * d + 1]));
      }
      lds_st<v2u>(xa + i * 128, (v2u){o[0], o[1]});
      lds_st<v2u>(xa + i * 128 + 4 * XT_P, (v2u){o[2], o[3]});
    }
    MEMFENCE();
#pragma unroll
    for (int i = 0; i < 4; ++i) {
      const u32 r = row0 + 2 * i + (lane >> 5);
      const v4u t = lds_ld<v4u>(L_XT + (2 * i + (lane >> 5)) * XT_P + qd * 512 + (lane & 31) * 16);
      st16<0>(t, ro, ((r == i0) | (r == i1)) ? OOB : r * 2048 + qd * 512 + (lane & 31) * 16);
    }
  }
}

typedef u32 v16u __attribute__((ext_vector_type(16)));
typedef u32 v8u __attribute__((ext_vector_type(8)));
#define ENDW() do { STAMP_FLUSH(); asm volatile("s_endpgm" ::: "memory"); } while (0)

__global__ void __launch_bounds__(512, 2) mega(Params p_unused) {
  extern __shared__ __attribute__((aligned(16))) char smem[];
  u64 pc0;
  v16u ka;
  v8u kb;
  u64 t64;
  asm volatile("s_load_dwordx16 %1, %4, 0x0\n\ts_load_dwordx8 %2, %4, 0x40\n\ts_memrealtime %3\n\ts_getpc_b64 %0"
               : "=&s"(pc0), "=&s"(ka), "=&s"(kb), "=&s"(t64) : "s"(__builtin_amdgcn_kernarg_segment_ptr()));
  const int tid = threadIdx.x;
  int wave = __builtin_amdgcn_readfirstlane(tid >> 6);
  const int lane = tid & 63;
  const int b = blockIdx.x, xcd = b & 7, rk = b >> 3;
  const u32 codepf = ld4<0>(mkrs((const void*)(pc0 & ~255ull), CODE_KB * 1024), (rk & 3) * 10240 + wave * 1280 + lane * 128 + (lane >= 10 ? OOB : 0u));
  SCHED();
  const int L = xcd * 32 + rk;
  lds_st<u32>(L_CTL + (tid & 31) * 4, 0u);
  const int jw = wave & 3, cg = L >> 4, rb = L & 15, ch = 4 * L + jw;
  const u32 row0 = (u32)((((L >> 1) & 3) * 32 + (L >> 3)) * 16 + (L & 1) * 8);
  u32 toff = (u32)((jw * 512 + rb * 32 + (lane >> 3) * 4) * 2048 + cg * 128 + (lane & 7) * 16);
  u32 qoff = (row0 + 2 * jw + (lane >> 5)) * 2048 + (lane & 31) * 16;
  asm volatile("" : "+v"(toff), "+v"(qoff));
  asm volatile("s_waitcnt lgkmcnt(0)" : "+s"(ka), "+s"(kb), "+s"(t64) :: "memory");
  const u32 t0 = (u32)t64;
  STAMP_DECL;
  __builtin_amdgcn_s_barrier();
  SCHED();
  Params p;
#define KP(v, i) (((u64)v[2 * (i) + 1] << 32) | v[2 * (i)])
  p.q = (const u16*)KP(ka, 0); p.k = (const u16*)KP(ka, 1); p.v = (const u16*)KP(ka, 2); p.shk = (const u16*)KP(ka, 3);
  p.ssmk = (const u16*)KP(ka, 4); p.ssmD = (const u16*)KP(ka, 5); p.wr = (const u16*)KP(ka, 6); p.wg = (const u16*)KP(ka, 7);
  p.wu = (const u16*)KP(kb, 0); p.wd = (const u16*)KP(kb, 1); p.out = (u16*)KP(kb, 2); p.ws = (char*)KP(kb, 3);
#undef KP
  const rsrc_t rq = mkrs(p.q, S * D * 2);
  v4u U[19];
  if (wave >= 4) {
#pragma unroll
    for (int i = 0; i < 4; ++i) U[i] = ld16<POL_IN>(rq, qoff + i * 512);
#pragma unroll
    for (int i = 0; i < 4; ++i) U[4 + i] = ld16<0>(mkrs(p.wr, D * 2), (lane & 31) * 16 + i * 512);
    SCHED();
  }
  asm volatile("" : "+s"(wave));
  if (wave < 4) {
    const rsrc_t rk_ = mkrs(p.k, S * D * 2), rv = mkrs(p.v, S * D * 2), rs = mkrs(p.shk, D * 8);
    if (jw) { SCHED(); hold(t0, T_H); }
#pragma unroll
    for (int i = 0; i < 4; ++i) U[15 + i] = ld16<POL_IN>(mkrs(p.ssmk, S * D * 2), ch * 4096 + (i * 64 + lane) * 16);
    SCHED();
    if (jw) { SCHED(); hold(t0, jw == 1 ? T_S1 : jw == 2 ? T_S2 : T_S3); }
#pragma unroll
    for (int i = 0; i < 4; ++i) U[3 + i] = ld16<POL_IN>(rk_, toff + i * 2048);
#pragma unroll
    for (int i = 0; i < 4; ++i) U[7 + i] = ld16<POL_IN>(rv, toff + i * 2048);
#pragma unroll
    for (int i = 0; i < 3; ++i) U[i] = ld16<0>(rk_, toff - (3 - i) * 2048);
#pragma unroll
    for (int i = 0; i < 4; ++i) U[11 + i] = ld16<0>(rs, (cg * 64 + (lane & 7) * 8) * 8 + i * 16);
    SCHED();
  }
  asm volatile("" : "+s"(wave));
  const rsrc_t rws = mkrs(p.ws, WS_END);
  const bool ffn = rk < 28;
  const int j = xcd * 28 + rk;
  u32 epoch = 0;
  if (wave >= 4) {
    epoch = *(const CAS u32*)(p.ws + WS_EPOCH);
    const v4u sent = {SENT, SENT, SENT, SENT};
    st16<SC1>(sent, rws, WS_YX + (96 + jw * 8 + (lane >> 3)) * YSTR + L * 128 + (lane & 7) * 16);
    st16<SC1>(sent, rws, (!ffn || jw == 3) ? OOB : jw == 2 ? WS_GU + j * 1024 + lane * 16 : WS_DN + j * 2048 + jw * 1024 + lane * 16);
    SCHED();
    asm volatile("" : "+s"(epoch));
    if (jw == 3) lds_stv<v2u>(C_TAG, (v2u){1u, epoch + 1u});
  __builtin_amdgcn_s_setprio(LNPRIO);
  STAMP(1);
  {
    float sx = 0.f, sq = 0.f, sr = 0.f, sw = 0.f;
#pragma unroll
    for (int i = 0; i < 16; ++i) {
      const u32 a = U[i >> 2][i & 3], wv = U[4 + (i >> 2)][i & 3];
      sx = dot2(a, 0x3f803f80u, sx); sq = dot2(a, a, sq); sr = dot2(a, wv, sr); sw = dot2(wv, 0x3f803f80u, sw);
    }
#define RED4(ctl) "v_add_f32_dpp %0, %0, %0 " ctl "\n\tv_add_f32_dpp %1, %1, %1 " ctl "\n\tv_add_f32_dpp %2, %2, %2 " ctl "\n\tv_add_f32_dpp %3, %3, %3 " ctl "\n\t"
    asm("s_nop 2\n\t" RED4("row_shr:1 row_mask:0xf bank_mask:0xf bound_ctrl:1") RED4("row_shr:2 row_mask:0xf bank_mask:0xf bound_ctrl:1")
        RED4("row_shr:4 row_mask:0xf bank_mask:0xf bound_ctrl:1") RED4("row_shr:8 row_mask:0xf bank_mask:0xf bound_ctrl:1")
        RED4("row_bcast:15 row_mask:0xa bank_mask:0xf") : "+v"(sx), "+v"(sq), "+v"(sr), "+v"(sw));
#undef RED4
    const float mu = sx * (1.f / D);
    const u32 mv = pk2(mu, __builtin_fmaxf(__builtin_fmaf(-mu, mu, sq * (1.f / D)), 0.f));
    const float sd = rbf(__builtin_amdgcn_sqrtf(rbf(hi2f(mv) + 1e-5f)));
    const float rs = __builtin_amdgcn_rcpf(sd);
    const u32 rp = pk2(0.f, __builtin_fmaf(-lo2f(mv), sw, sr) * rs);
    u32 key = (rp ^ ((u32)((int)rp >> 31) | 0x80000000u)) & 0xFFFF0000u;
    key |= 2047u - (row0 + 2 * jw + (lane >> 5));
    u32 ms = (mv & 0xFFFFu) | asu(sd);
    asm volatile("" : "+v"(ms), "+v"(key) : "v"(codepf));
#pragma unroll
    for (int i = 0; i < 4; ++i) lds_st<v4u>(L_XT + (2 * jw + (lane >> 5)) * XT_P + (lane & 31) * 16 + i * 512, U[i]);
    VMCNT(0);
    if ((lane & 31) == 31) {
      lds_st<f32x2>(L_RN + (2 * jw + (lane >> 5)) * 8, (f32x2){rs, -lo2f(mv) * rs});
      lds_st<u32>(L_LN + jw * 16 + (lane >> 5) * 4, ms);
      lds_st<u32>(L_LN + jw * 16 + 8 + (lane >> 5) * 4, key);
      MEMFENCE();
      lds_stv<u32>(C_LN + jw * 4, 1u);
    }
    MEMFENCE();
  }
    __builtin_amdgcn_s_setprio(0);
    STAMP(2);
  }
  asm volatile("" : "+s"(wave));

  if (wave < 4) {
    const int w = jw;
    const int g = lane & 7, rr = lane >> 3;
    v2u hn[4];
    const u32 Dw = *(const CAS u32*)(p.ssmD + (ch & ~1));
    SCHED();
    const u32 hb = L_TS + w * 4224;
    u32 tw = hb + g * 528 + (rr >> 1) * 16 + ((rr & 1) ? 0 : 8), tr_ = hb + (lane >> 5) * 528 + (lane & 31) * 16;
    u32 ko = WS_KX + (cg * 64 + w * 16 + rb) * 4096 + lane * 16;
    asm volatile("" : "+v"(tw), "+v"(tr_), "+v"(ko));
    if (lane < 32) lds_st<v4u>(L_UB + w * UB_CH + lane * 16, (v4u){0, 0, 0, 0});
    auto copies = [&]() {
    SCHED();
    const u32 hc = L_HC + w * HC_CH;
#pragma unroll
    for (int i = 0; i < 4; ++i) {
      const u32 nx = i < 3 ? rfl(U[i < 3 ? 16 + i : 18].x) : 0u, ny = i < 3 ? rfl(U[i < 3 ? 16 + i : 18].y) : 0u;
      hn[i].x = __builtin_amdgcn_update_dpp(nx, U[15 + i].x, 0x130, 0xf, 0xf, false);
      hn[i].y = __builtin_amdgcn_update_dpp(ny, U[15 + i].y, 0x130, 0xf, 0xf, false);
    }
#pragma unroll
    for (int i = 0; i < 4; ++i) {
      const v4u d = U[15 + i];
      const u32 e0 = __builtin_amdgcn_alignbit(d.y, d.x, 16), e1 = __builtin_amdgcn_alignbit(d.z, d.y, 16),
                e2 = __builtin_amdgcn_alignbit(d.w, d.z, 16), e3 = __builtin_amdgcn_alignbit(hn[i].x, d.w, 16),
                e4 = __builtin_amdgcn_alignbit(hn[i].y, hn[i].x, 16);
      const u32 a = hc + 32 + (i * 64 + lane) * 16;
      lds_st<v4u>(a, d);
      lds_st<v4u>(a + HC_COPY, (v4u){e0, e1, e2, e3});
      lds_st<v4u>(a + 2 * HC_COPY, (v4u){d.y, d.z, d.w, hn[i].x});
      lds_st<v4u>(a + 3 * HC_COPY, (v4u){e1, e2, e3, e4});
    }
    {
      const u32 s0 = rfl(U[15].x), s1 = rfl(U[15].y);
      const int cb = lane & 3;
      const u32 w3 = cb == 0 ? 0u : cb == 1 ? (s0 << 16) : cb == 2 ? s0 : ((s0 >> 16) | (s1 << 16));
      const v4u z = {0, 0, lane == 7 ? (s0 << 16) : 0u, (lane & 4) ? w3 : 0u};
      if (lane < 8) lds_st<v4u>(hc + cb * HC_COPY + (lane >> 2) * 16, z);
    }
    MEMFENCE();
    SCHED();
    };
    const v4u kc = chk_consts(lane);
    if (w) copies();
    const u32 tag = get_tag();
    STAMP(1);
    VMCNT(0);
    asm volatile("" :: "v"(codepf));
    STAMP(2);
#pragma unroll
    for (int c = 0; c < 8; ++c) {
      const u32 wa = U[11 + (c >> 1)][(c & 1) * 2], wb = U[11 + (c >> 1)][(c & 1) * 2 + 1];
      const u32 sel = (c & 1) ? 0x07060302u : 0x05040100u;
      u32 P[6];
      float ks[4], vf[4];
#pragma unroll
      for (int i = 0; i < 6; ++i) P[i] = __builtin_amdgcn_perm(U[i][c >> 1], U[i + 1][c >> 1], sel);
#pragma unroll
      for (int i = 0; i < 4; ++i) { const u32 vx = U[7 + i][c >> 1]; vf[i] = (c & 1) ? hi2f(vx) : lo2f(vx); }
      asm("v_dot2_f32_bf16 %0, %4, %11, 0\n\tv_dot2_f32_bf16 %1, %5, %11, 0\n\tv_dot2_f32_bf16 %2, %6, %11, 0\n\tv_dot2_f32_bf16 %3, %7, %11, 0\n\t"
          "v_dot2_f32_bf16 %0, %6, %10, %0\n\tv_dot2_f32_bf16 %1, %7, %10, %1\n\tv_dot2_f32_bf16 %2, %8, %10, %2\n\tv_dot2_f32_bf16 %3, %9, %10, %3\n\t"
          "v_mul_f32 %0, %0, %12\n\tv_mul_f32 %1, %1, %13\n\tv_mul_f32 %2, %2, %14\n\tv_mul_f32 %3, %3, %15"
          : "=&v"(ks[0]), "=&v"(ks[1]), "=&v"(ks[2]), "=&v"(ks[3])
          : "v"(P[0]), "v"(P[1]), "v"(P[2]), "v"(P[3]), "v"(P[4]), "v"(P[5]), "v"(wa), "v"(wb), "v"(vf[0]), "v"(vf[1]), "v"(vf[2]), "v"(vf[3]));
      lds_st<v2u>(tw + c * 64, (v2u){pk2(ks[3], ks[2]), pk2(ks[1], ks[0])});
    }
    MEMFENCE();
    {
      v4u d[4];
#pragma unroll
      for (int i = 0; i < 4; ++i) d[i] = lds_ld<v4u>(tr_ + i * 1056);
#pragma unroll
      for (int i = 0; i < 4; ++i) st16<SC1>(d[i], rws, ko + i * 1024);
      SCHED();
      st4<SC1>(chk_make(d, kc, lane) + tag * 0x9E3779B1u, rws, WS_KVF + (cg * 64 + w * 16 + rb) * 256 + lane * 4);
      lds_stv<u32>(C_PUB + w * 4, now() | 1u);
    }
    MEMFENCE();
    STAMP(3);
    if (w == 0) copies();
    STAMP(4);
    SCHED();
    Conv cv;
    cv.setup(w, lane, (ch & 1) ? (Dw >> 16) : (Dw & 0xFFFFu));
    STAMP(5);
    lds_wait_nz(C_STG);
    STAMP(6);
    cv.stream();
    STAMP(7);
    epilogue(rws, mkrs(p.out, S * D * 2), row0, lane);
    STAMP(8);
    ENDW();
  }

  v4u R[48];
  const rsrc_t rw = mkrs(jw == 0 ? p.wg : p.wu, D * F * 2);
  const u32 wo = ((j & 3) * 256 + (lane >> 3) * 8) * (F * 2) + (j >> 2) * 128 + (lane & 7) * 16;

  if (jw < 2) {
    if (ffn) {
    const int q4 = lane >> 4, N = lane & 15;
    const rsrc_t rd = mkrs(p.wd, D * F * 2);
    const u32 dofs = ((j >> 2) * 64 + q4 * 8) * 2048 + (j & 3) * 512 + jw * 256 + N * 16;
#pragma unroll
    for (int i = 0; i < 32; ++i) {
      if (i >= WIN) VMCNT(WIN - 1);
      if ((i & 1) == 0) hold(t0, T_W0 + i * T_WS);
      R[i] = ld16<POL_W>(rw, wo + ((i >> 3) * 64 + (i & 7)) * (F * 2));
      SCHED();
      if (i >= WIN + 8 && ((i - WIN) & 7) == 0) { perm8(&R[i - WIN - 8]); SCHED(); }
    }
#pragma unroll
    for (int i = 32; i < 40; ++i) R[i] = ld16<POL_W>(rd, dofs + (i & 7) * 2048);
    SCHED();
#pragma unroll
    for (int bk = (31 - WIN) >> 3; bk < 3; ++bk) perm8(&R[bk * 8]);
    VMCNT(8);
#pragma unroll
    for (int i = 40; i < 48; ++i) R[i] = ld16<POL_W>(rd, dofs + (32 + (i & 7)) * 2048);
    SCHED();
    perm8(&R[24]);
#pragma unroll
    for (int i = 0; i < 32; ++i) asm volatile("" : "+v"(R[i]));
    u32 ax = L_XS + ((lane >> 1) & 1) * 512 + q4 * 32 + (lane & 1) * 16;
    const bool kap = (lane >> 3) & 1;
    u32 oo = lane < 32 ? WS_GU + j * 1024 + jw * 512 + lane * 16 : OOB;
    asm volatile("" : "+v"(ax), "+v"(oo));
    STAMP(3);
    lds_wait_nz(C_SEL);
    __builtin_amdgcn_s_setprio(2);
    STAMP(4);
    {
      f32x4 acc[8];
#pragma unroll
      for (int c = 0; c < 8; ++c) acc[c] = (f32x4){0, 0, 0, 0};
#pragma unroll
      for (int bk = 0; bk < 4; ++bk) {
        const v4u X = lds_ld<v4u>(ax + bk * 128);
#pragma unroll
        for (int c = 0; c < 8; ++c) acc[c] = mfma(X, R[bk * 8 + c], acc[c]);
      }
      u32 o[8];
#pragma unroll
      for (int cc = 0; cc < 4; ++cc)
#pragma unroll
        for (int t = 0; t < 2; ++t) {
          const float keep = kap ? acc[4 + cc][2 * t + 1] : acc[cc][2 * t], send = kap ? acc[cc][2 * t + 1] : acc[4 + cc][2 * t];
          o[cc * 2 + t] = nosent(keep + dppf<0x128, 0xf>(send));
        }
      const bool hi = lane & 16;
      st16<SC1>((v4u){hi ? o[4] : o[0], hi ? o[5] : o[1], hi ? o[6] : o[2], hi ? o[7] : o[3]}, rws, oo);
    }
    const u32 tpub = now();
    STAMP(5);
    SCHED();
    perm8(&R[32]);
    perm8(&R[40]);
#pragma unroll
    for (int i = 32; i < 48; ++i) asm volatile("" : "+v"(R[i]));
    const u32 ha = L_HA + jw * 256;
    u32 ah[2][2];
#pragma unroll
    for (int hf = 0; hf < 2; ++hf)
#pragma unroll
      for (int ks = 0; ks < 2; ++ks) {
        ah[hf][ks] = (N >> 1) == hf ? ha + (N & 1) * 128 + ks * 64 + q4 * 16 : C_ZERO;
        asm volatile("" : "+v"(ah[hf][ks]));
      }
    u32 o2 = WS_DN + j * 2048 + jw * 1024 + lane * 16;
    u32 po = WS_GU + (j & ~3) * 1024 + lane * 8;
    u32 hw = ha + (((lane >> 1) & 7) * 8 + ((lane >> 4) & 1) * 4 + (lane >> 5) * 2 + (lane & 1)) * 2;
    u32 dw = L_DS + jw * 1024 + q4 * 256 + N * 16;
    asm volatile("" : "+v"(o2), "+v"(po), "+v"(hw), "+v"(dw));
    STAMP(6);
    hold(tpub, T_2A);
    v2u pv[8];
    SPIN(SPIN_G) {
      MEMFENCE();
      u32 mx = 0;
#pragma unroll
      for (int i = 0; i < 8; ++i) pv[i] = ld8<SC1>(rws, po + i * 512);
#pragma unroll
      for (int i = 0; i < 8; ++i) mx = umax(mx, umax(pv[i].x, pv[i].y));
      if (wave_all(mx != SENT)) break;
      __builtin_amdgcn_s_sleep(2);
    }
    STAMP(7);
    const float g0 = (asf(pv[0].x) + asf(pv[2].x)) + (asf(pv[4].x) + asf(pv[6].x)), g1 = (asf(pv[0].y) + asf(pv[2].y)) + (asf(pv[4].y) + asf(pv[6].y));
    const float u0 = (asf(pv[1].x) + asf(pv[3].x)) + (asf(pv[5].x) + asf(pv[7].x)), u1 = (asf(pv[1].y) + asf(pv[3].y)) + (asf(pv[5].y) + asf(pv[7].y));
    const u32 gp = pk2(g0, g1), up = pk2(u0, u1);
    const float ga = lo2f(gp), gb = hi2f(gp);
    const u32 sp = pk2(ga * __builtin_amdgcn_rcpf(1.f + __expf(-ga)), gb * __builtin_amdgcn_rcpf(1.f + __expf(-gb)));
    const u32 hp = pk2(lo2f(sp) * lo2f(up), hi2f(sp) * hi2f(up));
    lds_st<u16>(hw, (u16)hp);
    lds_st<u16>(hw + 128, (u16)(hp >> 16));
    MEMFENCE();
    f32x4 acc[4];
#pragma unroll
    for (int c = 0; c < 4; ++c) acc[c] = (f32x4){0, 0, 0, 0};
#pragma unroll
    for (int ks = 0; ks < 2; ++ks)
#pragma unroll
      for (int hf = 0; hf < 2; ++hf) {
        const v4u Hb = lds_ld<v4u>(ah[hf][ks]);
#pragma unroll
        for (int cc = 0; cc < 4; ++cc) acc[cc] = mfma(R[32 + ks * 8 + hf * 4 + cc], Hb, acc[cc]);
      }
    if (N < 4) {
#pragma unroll
      for (int r = 0; r < 4; ++r) lds_st<f32x4>(dw + r * 64, (f32x4){acc[0][r], acc[1][r], acc[2][r], acc[3][r]});
    }
    MEMFENCE();
    {
      const f32x4 d = lds_ld<f32x4>(L_DS + jw * 1024 + lane * 16);
      st16<SC1>((v4u){nosent(d[0]), nosent(d[1]), nosent(d[2]), nosent(d[3])}, rws, o2);
    }
    STAMP(8);
    } else {
      hold(t0, T_NF);
    }
    const u32 tdn = now();
    if (jw == 1) {
      __builtin_amdgcn_s_setprio(0);
      lds_wait_nz(C_YOK);
      epilogue(rws, mkrs(p.out, S * D * 2), row0, lane);
      ENDW();
    }
    lds_wait_nz(C_SEL);
    const v4u ks4 = lds_ldv<v4u>(C_SEL);
    const u32 kk = (lane & 1) ? ks4.z : ks4.y;
    const u32 frow = 2047u - (kk & 0x7FFu), kx = kk >> 16;
    const float rsel = lo2f((kx & 0x8000u) ? (kx ^ 0x8000u) : (kx ^ 0xFFFFu));
    float sig = rbf(__builtin_amdgcn_rcpf(1.f + __expf(-rsel)));
    const v2u xf = ld8<0>(rq, frow * 2048 + L * 8);
    const v4u sg = ld16<SC1>(rws, WS_REC + (frow >> 3) * 128 + 16 + ((frow >> 1) & 3) * 16);
    u32 pa = WS_DN + ((lane >> 1) * 4 + (L >> 6)) * 2048 + (L & 63) * 32 + (lane & 1) * 16;
    u32 pb_ = lane < 48 ? pa + 32 * 4 * 2048 : OOB;
    asm volatile("" : "+v"(pa), "+v"(pb_), "+v"(sig));
    hold(tdn, T_2B);
    v4u e0, e1;
    SPIN(SPIN_G) {
      MEMFENCE();
      e0 = ld16<SC1>(rws, pa); e1 = ld16<SC1>(rws, pb_);
      const u32 mx = umax(umax(umax(e0.x, e0.y), umax(e0.z, e0.w)), umax(umax(e1.x, e1.y), umax(e1.z, e1.w)));
      if (wave_all(mx != SENT)) break;
      __builtin_amdgcn_s_sleep(2);
    }
    STAMP(9);
    float y[4];
#pragma unroll
    for (int c = 0; c < 4; ++c) {
      const u32 ua = e0[c], ub_ = e1[c];
      float s = asf(ua) + asf(ub_);
      s += dppf<0x122, 0xf>(s); s += dppf<0x124, 0xf>(s); s += dppf<0x128, 0xf>(s);
      const auto r16 = __builtin_amdgcn_permlane16_swap(asu(s), asu(s), false, false);
      s = asf(r16[0]) + asf(r16[1]);
      const auto r32 = __builtin_amdgcn_permlane32_swap(asu(s), asu(s), false, false);
      y[c] = rbf(rbf(asf(r32[0]) + asf(r32[1])) * sig);
    }
    lds_wait_min4(C_OUT, (2047u - (umin(rfl(ks4.y) & 0x7FFu, rfl(ks4.z) & 0x7FFu))) >> 9);
    float o[4];
#pragma unroll
    for (int c = 0; c < 4; ++c) {
      const u32 msf = (frow & 1) ? sg.y : sg.x, xx = xf[c >> 1];
      const float x = (c & 1) ? hi2f(xx) : lo2f(xx);
      o[c] = __builtin_fmaf((x - lo2f(msf)) * __builtin_amdgcn_rcpf(hi2f(msf)), lo2f(lds_ld<u16>(L_OS + c * OS_CH + frow * 2)), x) + y[c];
    }
    st8<0>((v2u){pk2(o[0], o[1]), pk2(o[2], o[3])}, mkrs(p.out, S * D * 2), lane < 2 ? frow * 2048 + L * 8 : OOB);
    STAMP(10);
    ENDW();
  }
  if (jw == 2) {
    __builtin_amdgcn_s_setprio(2);
    const u32 tag = get_tag();
    {
      u32 aI = L_UB + ((lane >> 2) & 3) * UB_CH + 512 + (lane >> 4) * 64 + (lane & 3) * 16;
      u32 io = WS_KX + (cg * 64 + (lane >> 4)) * 4096 + rb * 256 + (lane & 15) * 16;
      u32 co = (lane & 12) ? OOB : WS_KVF + (cg * 64 + (lane & 3) * 4 + (lane >> 4)) * 256 + ((rb & 3) * 16 + (rb >> 2)) * 4;
      const v4u kc = chk_consts(lane);
      u32 tm = (lane & 12) ? 0u : tag * 0x9E3779B1u;
      asm volatile("" : "+v"(aI), "+v"(io), "+v"(co), "+v"(tm));
      v4u d[4][4];
      u32 c[4];
#define ISSUE(s_) do { \
        lds_wait_nz(C_PUB + s_ * 4); \
        hold(rfl(lds_ldv<u32>(C_PUB + s_ * 4)), T_KVP); \
        MEMFENCE(); \
        _Pragma("unroll") for (int i = 0; i < 4; ++i) d[s_][i] = ld16<SC1>(rws, io + (s_ * 16 + i * 4) * 4096); \
        c[s_] = ld4<SC1>(rws, co + s_ * 16 * 256); \
        SCHED(); } while (0)
#define LAND(s_) do { \
        SPIN(SPIN_G) { \
          _Pragma("unroll") for (int i = 0; i < 4; ++i) lds_st<v4u>(aI + s_ * 1024 + i * 256, d[s_][i]); \
          if (chk_ok(d[s_], kc, tm - c[s_])) break; \
          MEMFENCE(); \
          _Pragma("unroll") for (int i = 0; i < 4; ++i) d[s_][i] = ld16<SC1>(rws, io + (s_ * 16 + i * 4) * 4096); \
          c[s_] = ld4<SC1>(rws, co + s_ * 16 * 256); \
          VMCNT(0); \
        } \
        MEMFENCE(); \
        lds_stv<u32>(C_STG, s_ + 1); \
        STAMP(3 + s_); \
        SCHED(); } while (0)
      ISSUE(0); ISSUE(1); LAND(0); ISSUE(2); LAND(1); ISSUE(3); LAND(2); LAND(3);
#undef ISSUE
#undef LAND
    }
    STAMP(7);
    {
      const u32 st = (L >> 1) & 3;
      SPIN(SPIN_L) { if (rfl(lds_ldv<u32>(C_YP)) > st) break; __builtin_amdgcn_s_sleep(1); }
      MEMFENCE();
      hold(now(), st == 3 ? T_Y3 : T_Y);
      if (st < 3) {
        u32 go[4];
#pragma unroll
        for (int i = 0; i < 4; ++i) go[i] = WS_YF + (st * 256 + i * 64 + lane) * 128;
        SPIN(SPIN_G) {
          MEMFENCE();
          u32 f[4], pend = OOB;
#pragma unroll
          for (int i = 0; i < 4; ++i) f[i] = ld4<SC1>(rws, go[i]);
#pragma unroll
          for (int i = 0; i < 4; ++i) { go[i] = f[i] == tag ? OOB : go[i]; pend = umin(pend, go[i]); }
          if (wave_all(pend == OOB)) break;
          __builtin_amdgcn_s_sleep(SLP_KV);
        }
        MEMFENCE();
      }
      lds_stv<u32>(C_YOK, 1u);
    }
    STAMP(8);
    __builtin_amdgcn_s_setprio(0);
    epilogue(rws, mkrs(p.out, S * D * 2), row0, lane);
    STAMP(9);
    ENDW();
  }

  const u32 tag = epoch + 1u;
  __builtin_amdgcn_s_setprio(2);
  lds_wait_min4(C_LN, 0);
  {
    const v4u r4 = lds_ld<v4u>(L_LN + (lane & 3) * 16);
    u32 a1 = umax(r4.z, r4.w), a2 = umin(r4.z, r4.w);
    u32 b1 = dppu<0xB1, 0xf>(a1), b2 = dppu<0xB1, 0xf>(a2);
    a2 = umax(umin(a1, b1), umax(a2, b2)); a1 = umax(a1, b1);
    b1 = dppu<0x4E, 0xf>(a1); b2 = dppu<0x4E, 0xf>(a2);
    a2 = umax(umin(a1, b1), umax(a2, b2)); a1 = umax(a1, b1);
    const u32 f0 = lane == 4 ? a1 : r4.x, f1 = lane == 4 ? a2 : r4.y;
    const u32 zz = 0u;
    st16<SC1>((v4u){f0, f1, zz, (f0 + f1 + zz) ^ tag}, rws, lane < 8 ? WS_REC + (row0 >> 3) * 128 + (lane == 4 ? 0 : lane < 4 ? lane + 1 : lane) * 16 : OOB);
  }
  STAMP(3);
  hold(t0, T_REC);
  u32 k1 = 0, k2 = 0;
  {
    u32 go[4];
#pragma unroll
    for (int i = 0; i < 4; ++i) go[i] = WS_REC + (i * 64 + lane) * 128;
    SPIN(SPIN_G) {
      MEMFENCE();
      v4u gr[4];
#pragma unroll
      for (int i = 0; i < 4; ++i) gr[i] = ld16<SC1>(rws, go[i]);
      u32 pend = OOB;
#pragma unroll
      for (int i = 0; i < 4; ++i) {
        const bool ok = (((gr[i].x + gr[i].y + gr[i].z) ^ gr[i].w) == tag) & (go[i] != OOB);
        const u32 ka_ = ok ? gr[i].x : 0u, kb_ = ok ? gr[i].y : 0u;
        k2 = umax(med3u(k1, k2, ka_), kb_); k1 = umax(k1, ka_);
        go[i] = ok ? OOB : go[i];
        pend = umin(pend, go[i]);
      }
      if (wave_all(pend == OOB)) break;
      __builtin_amdgcn_s_sleep(SLP_REC);
    }
    MEMFENCE();
  }
  STAMP(4);
  {
    const u32 t1 = wave_max(k1);
    const u32 t2 = wave_max(k1 == t1 ? k2 : k1);
    const u32 myrow = 2047u - (((lane >> 5) ? t2 : t1) & 0x7FFu);
    const u32 so = WS_REC + (myrow >> 3) * 128 + 16 + ((myrow >> 1) & 3) * 16;
    const v4u xs = ld16<0>(rq, myrow * 2048 + (j & 3) * 512 + (lane & 31) * 16);
    v4u sg = ld16<SC1>(rws, so);
    st4<SC1>(tag, rws, (L == 0 && lane == 0) ? WS_EPOCH : OOB);
    SCHED();
    if (!wave_all(((sg.x + sg.y + sg.z) ^ sg.w) == tag)) {
      SPIN(SPIN_G) {
        MEMFENCE();
        sg = ld16<SC1>(rws, so);
        if (wave_all(((sg.x + sg.y + sg.z) ^ sg.w) == tag)) break;
      }
    }
    const u32 ms = (myrow & 1) ? sg.y : sg.x;
    const float rs = __builtin_amdgcn_rcpf(hi2f(ms)), nb = -lo2f(ms) * rs;
    v4u xn;
#pragma unroll
    for (int i = 0; i < 4; ++i) { const u32 xx = xs[i]; xn[i] = pk2(__builtin_fmaf(lo2f(xx), rs, nb), __builtin_fmaf(hi2f(xx), rs, nb)); }
    lds_st<v4u>(L_XS + lane * 16, xn);
    MEMFENCE();
    lds_stv<v4u>(C_SEL, (v4u){1u, t1, t2, 0u});
    MEMFENCE();
  }
  STAMP(5);
  {
    u32 ra = L_OS + ((lane >> 2) & 3) * OS_CH + ((lane >> 4) * 32 + (lane & 3) * 8) * 2;
    u32 yo = WS_YX + (lane >> 3) * YSTR + L * 128 + (lane & 7) * 16;
    u32 fo = lane < 8 ? WS_YF + L * 128 + lane * 16 : OOB;
    u32 c = 0, flagged = 0;
    SPIN(SPIN_L) {
      const v4u f = lds_ldv<v4u>(C_OUT);
      const u32 n = rfl(umin(umin(f.x, f.y), umin(f.z, f.w)));
      MEMFENCE();
      if (n > c) {
#pragma unroll 1
        for (; c < n; ++c) {
          v2u o[8];
#pragma unroll
          for (int i = 0; i < 8; ++i)
            o[i] = __builtin_bit_cast(v2u, __builtin_amdgcn_ds_read_tr16_b64_v4i16((LAS s16x4*)(size_t)(ra + (i >> 1) * 256 + (i & 1) * 8)));
#pragma unroll
          for (int i = 0; i < 4; ++i) st16<SC1>((v4u){o[2 * i].x, o[2 * i].y, o[2 * i + 1].x, o[2 * i + 1].y}, rws, yo + i * 8 * YSTR);
          ra += 1024; yo += 32 * YSTR;
        }
        if (c == 4) { lds_stv<u32>(C_YP + 0, 4u); STAMP(6); }
        continue;
      }
      if (flagged < c && flagged < 3) {
        VMCNT(0);
#pragma unroll 1
        for (; flagged < c && flagged < 3; ++flagged) { st16<SC1>((v4u){tag, tag, tag, tag}, rws, fo); fo += 256 * 128; }
        if (c < 4) lds_stv<u32>(C_YP, flagged);
        continue;
      }
      if (c == 4) break;
      __builtin_amdgcn_s_sleep(1);
    }
  }
  STAMP(7);
  STAMP_FLUSH();
}

__global__ void tiny(u32* w) { if (threadIdx.x == 0) w[512 + blockIdx.x] = now(); }
extern "C" void kernel_launch(void* const* d_in, const int* in_sizes, int n_in, void* d_out, int out_size, void* d_ws,
                              size_t ws_size, hipStream_t stream) {
  Params p;
  p.q = (const u16*)d_in[0]; p.k = (const u16*)d_in[1]; p.v = (const u16*)d_in[2];
  p.shk = (const u16*)d_in[3]; p.ssmk = (const u16*)d_in[4]; p.ssmD = (const u16*)d_in[5];
  p.wr = (const u16*)d_in[7]; p.wg = (const u16*)d_in[8]; p.wu = (const u16*)d_in[9]; p.wd = (const u16*)d_in[10];
  p.out = (u16*)d_out; p.ws = (char*)d_ws;
  (void)hipFuncSetAttribute((const void*)mega, hipFuncAttributeMaxDynamicSharedMemorySize, L_END);
  hipLaunchKernelGGL(mega, dim3(256), dim3(512), L_END, stream, p);
}
```

```cpp
#include <hip/hip_runtime.h>
#include <stdint.h>

typedef unsigned short u16;
typedef unsigned u32;
typedef unsigned long long u64;
typedef u32 v4u __attribute__((ext_vector_type(4)));
typedef u32 v2u __attribute__((ext_vector_type(2)));
typedef float f32x4 __attribute__((ext_vector_type(4)));
typedef float f32x2 __attribute__((ext_vector_type(2)));
typedef __bf16 bf16x8 __attribute__((ext_vector_type(8)));
typedef __bf16 bf16x2 __attribute__((ext_vector_type(2)));
typedef short s16x4 __attribute__((ext_vector_type(4)));
#define LAS __attribute__((address_space(3)))
#define CAS __attribute__((address_space(4)))
typedef __amdgpu_buffer_rsrc_t rsrc_t;

#define WIN 9
#define CODE_KB 37
#define SC1 16
#define NT 2
#define ADAPT 185
#define KSTR 32
constexpr u32 TAGK = 0x9E3779B1u;
#define DNE 8
#define T_S1 105
#define T_S2 205
#define T_S3 250
#define T_W0 268
#define T_WS 11
#define T_REC 398
#define T_YP 430
#define T_Y0 140
#define T_2A 70
#define T_2B 18
#define T_NF 945
#define T_Y 95
#define T_Y3 78
#define T_KVP 62
#define T_H 40
#define T_KV1 146
#define LNPRIO 1
#define SLP_REC 6

constexpr int S = 2048, D = 1024, F = 3584;
constexpr u32 SENT = 0xFFFFFFFFu;
constexpr u32 OOB = 0x80000000u;
constexpr u32 SPIN_G = 1u << 16;
constexpr u32 SPIN_L = 1u << 22;

constexpr u32 WS_EPOCH = 0;
constexpr u32 WS_REC = 4096;
constexpr u32 WS_KEY = WS_REC + 32768;
constexpr u32 WS_KVF = WS_KEY + 32768;
constexpr u32 WS_YC = WS_KVF + 262144;
constexpr u32 WS_GU = WS_YC + 524288;
constexpr u32 WS_DN = WS_GU + 229376;
constexpr u32 WS_KX = 2u << 20;
constexpr u32 YSTR = 35072;
constexpr u32 WS_YX = 6u << 20;
constexpr u32 WS_END = 12u << 20;

constexpr u32 HC_COPY = 4160, HC_CH = 4 * HC_COPY;
constexpr u32 L_HC = 0;
constexpr u32 UB_CH = 512 + 4096 + 64;
constexpr u32 L_UB = L_HC + 4 * HC_CH;
constexpr u32 OS_CH = 4224;
constexpr u32 L_OS = L_UB + 4 * UB_CH;
constexpr u32 XT_P = 1056;
constexpr u32 L_XT = L_OS + 4 * OS_CH;
constexpr u32 L_TS = L_XT + 16 * XT_P;
constexpr u32 L_XS = L_TS + 4 * 5120;
constexpr u32 L_HA = L_XS + 1024;
constexpr u32 L_PK = L_HA + 512;
constexpr u32 L_LN = L_PK + 16384;
constexpr u32 L_RN = L_LN + 64;
constexpr u32 L_FR = L_RN + 128;
constexpr u32 L_CTL = L_FR + 64;
constexpr u32 C_OUT = L_CTL;
constexpr u32 C_SEL = L_CTL + 16;
constexpr u32 C_LN = L_CTL + 32;
constexpr u32 C_PUB = L_CTL + 48;
constexpr u32 C_XS = L_CTL + 104;
constexpr u32 C_T0 = L_CTL + 108;
constexpr u32 C_STG = L_CTL + 112;
constexpr u32 C_CLR = L_CTL + 64;
constexpr u32 C_YP = L_CTL + 68;
constexpr u32 C_TAG = L_CTL + 72;
constexpr u32 C_ZERO = L_CTL + 160;
constexpr u32 C_YOK = L_CTL + 96;
constexpr u32 C_TKT = L_CTL + 100;
constexpr u32 C_HC = L_CTL + 128;
constexpr u32 L_END = L_CTL + 256;
static_assert(L_END <= 163840 && L_END > 81920 && WS_DN + 224 * 2048 <= WS_KX, "");

struct Params {
  const u16 *q, *k, *v, *shk, *ssmk, *ssmD, *wr, *wg, *wu, *wd;
  u16* out;
  char* ws;
};

__device__ __forceinline__ rsrc_t mkrs(const void* p, u32 bytes) {
  return __builtin_amdgcn_make_buffer_rsrc((void*)p, 0, bytes, 0x00020000);
}
template <int AUX> __device__ __forceinline__ v4u ld16(rsrc_t r, u32 off) { return __builtin_amdgcn_raw_buffer_load_b128(r, off, 0, AUX); }
template <int AUX> __device__ __forceinline__ v2u ld8(rsrc_t r, u32 off) { return __builtin_amdgcn_raw_buffer_load_b64(r, off, 0, AUX); }
template <int AUX> __device__ __forceinline__ u32 ld4(rsrc_t r, u32 off) { return __builtin_amdgcn_raw_buffer_load_b32(r, off, 0, AUX); }
template <int AUX> __device__ __forceinline__ void st16(v4u v, rsrc_t r, u32 off) { __builtin_amdgcn_raw_buffer_store_b128(v, r, off, 0, AUX); }
template <int AUX> __device__ __forceinline__ void st4(u32 v, rsrc_t r, u32 off) { __builtin_amdgcn_raw_buffer_store_b32(v, r, off, 0, AUX); }

__device__ __forceinline__ float asf(u32 x) { return __builtin_bit_cast(float, x); }
__device__ __forceinline__ u32 asu(float x) { return __builtin_bit_cast(u32, x); }
__device__ __forceinline__ float lo2f(u32 x) { return asf(x << 16); }
__device__ __forceinline__ float hi2f(u32 x) { return asf(x & 0xFFFF0000u); }
__device__ __forceinline__ u32 pk2(float lo, float hi) {
  f32x2 f = {lo, hi};
  return __builtin_bit_cast(u32, __builtin_convertvector(f, bf16x2));
}
__device__ __forceinline__ float fma1(float a, float b, float c) { float r; asm("v_fma_f32 %0, %1, %2, %3" : "=v"(r) : "v"(a), "v"(b), "v"(c)); return r; }
__device__ __forceinline__ float add1(float a, float b) { float r; asm("v_add_f32 %0, %1, %2" : "=v"(r) : "v"(a), "v"(b)); return r; }
__device__ __forceinline__ float mul1(float a, float b) { float r; asm("v_mul_f32 %0, %1, %2" : "=v"(r) : "v"(a), "v"(b)); return r; }
__device__ __forceinline__ float rbf(float f) { return lo2f(pk2(f, f)); }
__device__ __forceinline__ float dot2(u32 a, u32 b, float c) {
  return __builtin_amdgcn_fdot2_f32_bf16(__builtin_bit_cast(bf16x2, a), __builtin_bit_cast(bf16x2, b), c, false);
}
__device__ __forceinline__ f32x4 mfma(v4u a, v4u b, f32x4 c) {
  return __builtin_amdgcn_mfma_f32_16x16x32_bf16(__builtin_bit_cast(bf16x8, a), __builtin_bit_cast(bf16x8, b), c, 0, 0, 0);
}
#define SCHED() __builtin_amdgcn_sched_barrier(0)
#define VMCNT(n) do { SCHED(); asm volatile("s_waitcnt vmcnt(%0)" ::"n"(n) : "memory"); \
    __builtin_amdgcn_s_waitcnt(((n) & 15) | (7 << 4) | (15 << 8) | (((n) >> 4) << 14)); SCHED(); } while (0)
#define MEMFENCE() asm volatile("" ::: "memory")
#define SPIN(cap) _Pragma("unroll 1") for (u32 spins = 0; spins < (cap); ++spins)

template <int CTRL, int RM> __device__ __forceinline__ float dppf(float x) {
  return asf(__builtin_amdgcn_update_dpp(0u, asu(x), CTRL, RM, 0xf, false));
}
template <int CTRL, int RM> __device__ __forceinline__ u32 dppu(u32 x) { return __builtin_amdgcn_update_dpp(0u, x, CTRL, RM, 0xf, false); }
__device__ __forceinline__ u32 umax(u32 a, u32 b) { return a > b ? a : b; }
__device__ __forceinline__ u32 umin(u32 a, u32 b) { return a < b ? a : b; }
__device__ __forceinline__ u32 wave_max(u32 x) {
  asm("s_nop 1\n\tv_max_u32_dpp %0, %0, %0 row_shr:1 row_mask:0xf bank_mask:0xf\n\ts_nop 1\n\tv_max_u32_dpp %0, %0, %0 row_shr:2 row_mask:0xf bank_mask:0xf\n\t"
      "s_nop 1\n\tv_max_u32_dpp %0, %0, %0 row_shr:4 row_mask:0xf bank_mask:0xf\n\ts_nop 1\n\tv_max_u32_dpp %0, %0, %0 row_shr:8 row_mask:0xf bank_mask:0xf\n\t"
      "s_nop 1\n\tv_max_u32_dpp %0, %0, %0 row_bcast:15 row_mask:0xa bank_mask:0xf\n\ts_nop 1\n\tv_max_u32_dpp %0, %0, %0 row_bcast:31 row_mask:0xc bank_mask:0xf\n\ts_nop 1"
      : "+v"(x));
  return __builtin_amdgcn_readlane(x, 63);
}
__device__ __forceinline__ u32 med3u(u32 a, u32 b, u32 c) {
  u32 r; asm("v_med3_u32 %0, %1, %2, %3" : "=v"(r) : "v"(a), "v"(b), "v"(c)); return r;
}
__device__ __forceinline__ bool wave_all(bool p) { return __builtin_amdgcn_ballot_w64(p) == ~0ull; }
__device__ __forceinline__ u32 nosent(float f) { return umin(asu(f), SENT - 1u); }
__device__ __forceinline__ u32 rfl(u32 x) { return __builtin_amdgcn_readfirstlane(x); }

template <typename T> __device__ __forceinline__ T lds_ld(u32 a) { return *(LAS T*)(size_t)a; }
template <typename T> __device__ __forceinline__ T lds_ldv(u32 a) { return *(volatile LAS T*)(size_t)a; }
template <typename T> __device__ __forceinline__ void lds_st(u32 a, T v) { *(LAS T*)(size_t)a = v; }
template <typename T> __device__ __forceinline__ void lds_stv(u32 a, T v) { *(volatile LAS T*)(size_t)a = v; }
__device__ __forceinline__ void lds_wait_nz(u32 a) {
  SPIN(SPIN_L) {
    if (rfl(lds_ldv<u32>(a))) break;
    __builtin_amdgcn_s_sleep(1);
  }
  MEMFENCE();
}
__device__ __forceinline__ void lds_wait_min4(u32 a, u32 gt) {
  SPIN(SPIN_L) {
    const v4u f = lds_ldv<v4u>(a);
    if (rfl(umin(umin(f.x, f.y), umin(f.z, f.w))) > gt) break;
    __builtin_amdgcn_s_sleep(1);
  }
  MEMFENCE();
}

__device__ __forceinline__ void perm8(v4u* w) {
  v4u o[8];
#pragma unroll
  for (int c = 0; c < 8; ++c)
#pragma unroll
    for (int i = 0; i < 4; ++i)
      o[c][i] = __builtin_amdgcn_perm(w[2 * i + 1][c >> 1], w[2 * i][c >> 1], (c & 1) ? 0x07060302u : 0x05040100u);
#pragma unroll
  for (int c = 0; c < 8; ++c) w[c] = o[c];
}

__device__ __forceinline__ void mfma_acc(f32x4& c, v4u a, v4u b) {
  asm volatile("v_mfma_f32_16x16x32_bf16 %0, %1, %2, %0" : "+v"(c) : "v"(a), "v"(b));
}
__device__ __forceinline__ void mfma_one(f32x4& c, v4u a, v4u b) {
  asm volatile("v_mfma_f32_16x16x32_bf16 %0, %1, %2, 1.0" : "=&v"(c) : "v"(a), "v"(b));
}

struct Conv {
  v4u AD;
  f32x4 acc[8];
  u32 aA, aB, osw, flg, pk, park;
  v4u tl[4];

  template <int pp, int e> __device__ __forceinline__ void fin() {
    const f32x4 a = acc[2 * pp + e];
    v2u o = {pk2(a[0], a[1]), pk2(a[2], a[3])};
    lds_stv<v2u>(osw + (2 * pp + e) * 512, o);
    if constexpr (e == 1) lds_stv<u32>(flg, pp + 1);
  }
  __device__ __forceinline__ void need(u32 st) {
    if (rfl(pk) == 0) lds_wait_nz(C_STG + st * 4);
  }
  __device__ __forceinline__ void stream() {
    const v2u r0l = lds_ldv<v2u>(aA + 0); const v2u r0h = lds_ldv<v2u>(aA + 8); const v4u r0 = {r0l.x, r0l.y, r0h.x, r0h.y}; const v2u r8l = lds_ldv<v2u>(aA + 512); const v2u r8h = lds_ldv<v2u>(aA + 520); const v4u r8 = {r8l.x, r8l.y, r8h.x, r8h.y}; const v2u r16l = lds_ldv<v2u>(aA + 1024); const v2u r16h = lds_ldv<v2u>(aA + 1032); const v4u r16 = {r16l.x, r16l.y, r16h.x, r16h.y}; const v2u r24l = lds_ldv<v2u>(aA + 1536); const v2u r24h = lds_ldv<v2u>(aA + 1544); const v4u r24 = {r24l.x, r24l.y, r24h.x, r24h.y}; const v2u r32l = lds_ldv<v2u>(aA + 2048); const v2u r32h = lds_ldv<v2u>(aA + 2056); const v4u r32 = {r32l.x, r32l.y, r32h.x, r32h.y}; const v2u a2l = lds_ldv<v2u>(aA + 2560); const v2u a2h = lds_ldv<v2u>(aA + 2568); const v4u a2 = {a2l.x, a2l.y, a2h.x, a2h.y}; const v2u a3l = lds_ldv<v2u>(aA + 3072); const v2u a3h = lds_ldv<v2u>(aA + 3080); const v4u a3 = {a3l.x, a3l.y, a3h.x, a3h.y}; const v2u a4l = lds_ldv<v2u>(aA + 3584); const v2u a4h = lds_ldv<v2u>(aA + 3592); const v4u a4 = {a4l.x, a4l.y, a4h.x, a4h.y}; lds_wait_nz(C_STG); const v4u b1 = lds_ldv<v4u>(aB + 448); const v4u b5 = lds_ldv<v4u>(aB + 960); mfma_one(acc[0], r0, b1); SCHED();
    mfma_acc(acc[0], AD, b1); SCHED();
    mfma_one(acc[1], r8, b1); SCHED();
    mfma_one(acc[2], r16, b1); SCHED();
    mfma_one(acc[3], r24, b1); SCHED();
    const v4u b6 = lds_ldv<v4u>(aB + 384); mfma_one(acc[4], r32, b1); SCHED();
    const v2u r1l = lds_ldv<v2u>(aA + 64); const v2u r1h = lds_ldv<v2u>(aA + 72); const v4u r1 = {r1l.x, r1l.y, r1h.x, r1h.y}; mfma_one(acc[5], a2, b1); SCHED();
    const v2u r9l = lds_ldv<v2u>(aA + 576); mfma_one(acc[6], a3, b1); SCHED();
    const v2u r9h = lds_ldv<v2u>(aA + 584); const v4u r9 = {r9l.x, r9l.y, r9h.x, r9h.y}; mfma_one(acc[7], a4, b1); SCHED();
    const v2u r17l = lds_ldv<v2u>(aA + 1088); const v2u r17h = lds_ldv<v2u>(aA + 1096); const v4u r17 = {r17l.x, r17l.y, r17h.x, r17h.y}; mfma_acc(acc[1], r0, b5); SCHED();
    const v2u r25l = lds_ldv<v2u>(aA + 1600); mfma_acc(acc[1], AD, b5); SCHED();
    const v2u r25h = lds_ldv<v2u>(aA + 1608); const v4u r25 = {r25l.x, r25l.y, r25h.x, r25h.y}; mfma_acc(acc[2], r8, b5); SCHED();
    const v2u r33l = lds_ldv<v2u>(aA + 2112); const v2u r33h = lds_ldv<v2u>(aA + 2120); const v4u r33 = {r33l.x, r33l.y, r33h.x, r33h.y}; mfma_acc(acc[3], r16, b5); SCHED();
    const v2u a7l = lds_ldv<v2u>(aA + 2624); mfma_acc(acc[4], r24, b5); SCHED();
    const v2u a7h = lds_ldv<v2u>(aA + 2632); const v4u a7 = {a7l.x, a7l.y, a7h.x, a7h.y}; mfma_acc(acc[5], r32, b5); SCHED();
    const v2u a8l = lds_ldv<v2u>(aA + 3136); const v2u a8h = lds_ldv<v2u>(aA + 3144); const v4u a8 = {a8l.x, a8l.y, a8h.x, a8h.y}; mfma_acc(acc[6], a2, b5); SCHED();
    const v2u a9l = lds_ldv<v2u>(aA + 3648); mfma_acc(acc[7], a3, b5); SCHED();
    const v2u a9h = lds_ldv<v2u>(aA + 3656); const v4u a9 = {a9l.x, a9l.y, a9h.x, a9h.y}; mfma_acc(acc[0], r1, b6); SCHED();
    const v4u b10 = lds_ldv<v4u>(aB + 896); mfma_acc(acc[1], r9, b6); SCHED();
    mfma_acc(acc[2], r17, b6); SCHED();
    const v4u b11 = lds_ldv<v4u>(aB + 320); mfma_acc(acc[3], r25, b6); SCHED();
    const v2u r2l = lds_ldv<v2u>(aA + 128); const v2u r2h = lds_ldv<v2u>(aA + 136); const v4u r2 = {r2l.x, r2l.y, r2h.x, r2h.y}; mfma_acc(acc[4], r33, b6); SCHED();
    const v2u r10l = lds_ldv<v2u>(aA + 640); mfma_acc(acc[5], a7, b6); SCHED();
    const v2u r10h = lds_ldv<v2u>(aA + 648); const v4u r10 = {r10l.x, r10l.y, r10h.x, r10h.y}; mfma_acc(acc[6], a8, b6); SCHED();
    const v2u r18l = lds_ldv<v2u>(aA + 1152); const v2u r18h = lds_ldv<v2u>(aA + 1160); const v4u r18 = {r18l.x, r18l.y, r18h.x, r18h.y}; mfma_acc(acc[7], a9, b6); SCHED();
    const v2u r26l = lds_ldv<v2u>(aA + 1664); mfma_acc(acc[1], r1, b10); SCHED();
    const v2u r26h = lds_ldv<v2u>(aA + 1672); const v4u r26 = {r26l.x, r26l.y, r26h.x, r26h.y}; mfma_acc(acc[2], r9, b10); SCHED();
    const v2u r34l = lds_ldv<v2u>(aA + 2176); const v2u r34h = lds_ldv<v2u>(aA + 2184); const v4u r34 = {r34l.x, r34l.y, r34h.x, r34h.y}; mfma_acc(acc[3], r17, b10); SCHED();
    const v2u a12l = lds_ldv<v2u>(aA + 2688); mfma_acc(acc[4], r25, b10); SCHED();
    const v2u a12h = lds_ldv<v2u>(aA + 2696); const v4u a12 = {a12l.x, a12l.y, a12h.x, a12h.y}; mfma_acc(acc[5], r33, b10); SCHED();
    const v2u a13l = lds_ldv<v2u>(aA + 3200); const v2u a13h = lds_ldv<v2u>(aA + 3208); const v4u a13 = {a13l.x, a13l.y, a13h.x, a13h.y}; mfma_acc(acc[6], a7, b10); SCHED();
    const v2u a14l = lds_ldv<v2u>(aA + 3712); mfma_acc(acc[7], a8, b10); SCHED();
    const v2u a14h = lds_ldv<v2u>(aA + 3720); const v4u a14 = {a14l.x, a14l.y, a14h.x, a14h.y}; mfma_acc(acc[0], r2, b11); SCHED();
    const v4u b15 = lds_ldv<v4u>(aB + 832); mfma_acc(acc[1], r10, b11); SCHED();
    mfma_acc(acc[2], r18, b11); SCHED();
    const v4u b16 = lds_ldv<v4u>(aB + 256); mfma_acc(acc[3], r26, b11); SCHED();
    const v2u r3l = lds_ldv<v2u>(aA + 192); const v2u r3h = lds_ldv<v2u>(aA + 200); const v4u r3 = {r3l.x, r3l.y, r3h.x, r3h.y}; mfma_acc(acc[4], r34, b11); SCHED();
    const v2u r11l = lds_ldv<v2u>(aA + 704); mfma_acc(acc[5], a12, b11); SCHED();
    const v2u r11h = lds_ldv<v2u>(aA + 712); const v4u r11 = {r11l.x, r11l.y, r11h.x, r11h.y}; mfma_acc(acc[6], a13, b11); SCHED();
    const v2u r19l = lds_ldv<v2u>(aA + 1216); const v2u r19h = lds_ldv<v2u>(aA + 1224); const v4u r19 = {r19l.x, r19l.y, r19h.x, r19h.y}; mfma_acc(acc[7], a14, b11); SCHED();
    const v2u r27l = lds_ldv<v2u>(aA + 1728); mfma_acc(acc[1], r2, b15); SCHED();
    const v2u r27h = lds_ldv<v2u>(aA + 1736); const v4u r27 = {r27l.x, r27l.y, r27h.x, r27h.y}; mfma_acc(acc[2], r10, b15); SCHED();
    const v2u r35l = lds_ldv<v2u>(aA + 2240); const v2u r35h = lds_ldv<v2u>(aA + 2248); const v4u r35 = {r35l.x, r35l.y, r35h.x, r35h.y}; mfma_acc(acc[3], r18, b15); SCHED();
    const v2u a17l = lds_ldv<v2u>(aA + 2752); mfma_acc(acc[4], r26, b15); SCHED();
    const v2u a17h = lds_ldv<v2u>(aA + 2760); const v4u a17 = {a17l.x, a17l.y, a17h.x, a17h.y}; mfma_acc(acc[5], r34, b15); SCHED();
    const v2u a18l = lds_ldv<v2u>(aA + 3264); const v2u a18h = lds_ldv<v2u>(aA + 3272); const v4u a18 = {a18l.x, a18l.y, a18h.x, a18h.y}; mfma_acc(acc[6], a12, b15); SCHED();
    const v2u a19l = lds_ldv<v2u>(aA + 3776); mfma_acc(acc[7], a13, b15); SCHED();
    const v2u a19h = lds_ldv<v2u>(aA + 3784); const v4u a19 = {a19l.x, a19l.y, a19h.x, a19h.y}; mfma_acc(acc[0], r3, b16); SCHED();
    const v4u b20 = lds_ldv<v4u>(aB + 768); mfma_acc(acc[1], r11, b16); SCHED();
    mfma_acc(acc[2], r19, b16); SCHED();
    const v4u b21 = lds_ldv<v4u>(aB + 192); mfma_acc(acc[3], r27, b16); SCHED();
    const v2u r4l = lds_ldv<v2u>(aA + 256); const v2u r4h = lds_ldv<v2u>(aA + 264); const v4u r4 = {r4l.x, r4l.y, r4h.x, r4h.y}; mfma_acc(acc[4], r35, b16); SCHED();
    const v2u r12l = lds_ldv<v2u>(aA + 768); mfma_acc(acc[5], a17, b16); SCHED();
    const v2u r12h = lds_ldv<v2u>(aA + 776); const v4u r12 = {r12l.x, r12l.y, r12h.x, r12h.y}; mfma_acc(acc[6], a18, b16); SCHED();
    const v2u r20l = lds_ldv<v2u>(aA + 1280); const v2u r20h = lds_ldv<v2u>(aA + 1288); const v4u r20 = {r20l.x, r20l.y, r20h.x, r20h.y}; mfma_acc(acc[7], a19, b16); SCHED();
    const v2u r28l = lds_ldv<v2u>(aA + 1792); mfma_acc(acc[1], r3, b20); SCHED();
    const v2u r28h = lds_ldv<v2u>(aA + 1800); const v4u r28 = {r28l.x, r28l.y, r28h.x, r28h.y}; mfma_acc(acc[2], r11, b20); SCHED();
    const v2u r36l = lds_ldv<v2u>(aA + 2304); const v2u r36h = lds_ldv<v2u>(aA + 2312); const v4u r36 = {r36l.x, r36l.y, r36h.x, r36h.y}; mfma_acc(acc[3], r19, b20); SCHED();
    const v2u a22l = lds_ldv<v2u>(aA + 2816); mfma_acc(acc[4], r27, b20); SCHED();
    const v2u a22h = lds_ldv<v2u>(aA + 2824); const v4u a22 = {a22l.x, a22l.y, a22h.x, a22h.y}; mfma_acc(acc[5], r35, b20); SCHED();
    const v2u a23l = lds_ldv<v2u>(aA + 3328); const v2u a23h = lds_ldv<v2u>(aA + 3336); const v4u a23 = {a23l.x, a23l.y, a23h.x, a23h.y}; mfma_acc(acc[6], a17, b20); SCHED();
    const v2u a24l = lds_ldv<v2u>(aA + 3840); mfma_acc(acc[7], a18, b20); SCHED();
    const v2u a24h = lds_ldv<v2u>(aA + 3848); const v4u a24 = {a24l.x, a24l.y, a24h.x, a24h.y}; mfma_acc(acc[0], r4, b21); SCHED();
    const v4u b25 = lds_ldv<v4u>(aB + 704); mfma_acc(acc[1], r12, b21); SCHED();
    mfma_acc(acc[2], r20, b21); SCHED();
    const v4u b26 = lds_ldv<v4u>(aB + 128); mfma_acc(acc[3], r28, b21); SCHED();
    const v2u r5l = lds_ldv<v2u>(aA + 320); const v2u r5h = lds_ldv<v2u>(aA + 328); const v4u r5 = {r5l.x, r5l.y, r5h.x, r5h.y}; mfma_acc(acc[4], r36, b21); SCHED();
    const v2u r13l = lds_ldv<v2u>(aA + 832); mfma_acc(acc[5], a22, b21); SCHED();
    const v2u r13h = lds_ldv<v2u>(aA + 840); const v4u r13 = {r13l.x, r13l.y, r13h.x, r13h.y}; mfma_acc(acc[6], a23, b21); SCHED();
    const v2u r21l = lds_ldv<v2u>(aA + 1344); const v2u r21h = lds_ldv<v2u>(aA + 1352); const v4u r21 = {r21l.x, r21l.y, r21h.x, r21h.y}; mfma_acc(acc[7], a24, b21); SCHED();
    const v2u r29l = lds_ldv<v2u>(aA + 1856); mfma_acc(acc[1], r4, b25); SCHED();
    const v2u r29h = lds_ldv<v2u>(aA + 1864); const v4u r29 = {r29l.x, r29l.y, r29h.x, r29h.y}; mfma_acc(acc[2], r12, b25); SCHED();
    const v2u r37l = lds_ldv<v2u>(aA + 2368); const v2u r37h = lds_ldv<v2u>(aA + 2376); const v4u r37 = {r37l.x, r37l.y, r37h.x, r37h.y}; mfma_acc(acc[3], r20, b25); SCHED();
    const v2u a27l = lds_ldv<v2u>(aA + 2880); mfma_acc(acc[4], r28, b25); SCHED();
    const v2u a27h = lds_ldv<v2u>(aA + 2888); const v4u a27 = {a27l.x, a27l.y, a27h.x, a27h.y}; mfma_acc(acc[5], r36, b25); SCHED();
    const v2u a28l = lds_ldv<v2u>(aA + 3392); const v2u a28h = lds_ldv<v2u>(aA + 3400); const v4u a28 = {a28l.x, a28l.y, a28h.x, a28h.y}; mfma_acc(acc[6], a22, b25); SCHED();
    const v2u a29l = lds_ldv<v2u>(aA + 3904); mfma_acc(acc[7], a23, b25); SCHED();
    const v2u a29h = lds_ldv<v2u>(aA + 3912); const v4u a29 = {a29l.x, a29l.y, a29h.x, a29h.y}; mfma_acc(acc[0], r5, b26); SCHED();
    const v4u b30 = lds_ldv<v4u>(aB + 640); mfma_acc(acc[1], r13, b26); SCHED();
    mfma_acc(acc[2], r21, b26); SCHED();
    const v4u b31 = lds_ldv<v4u>(aB + 64); mfma_acc(acc[3], r29, b26); SCHED();
    const v2u r6l = lds_ldv<v2u>(aA + 384); const v2u r6h = lds_ldv<v2u>(aA + 392); const v4u r6 = {r6l.x, r6l.y, r6h.x, r6h.y}; mfma_acc(acc[4], r37, b26); SCHED();
    const v2u r14l = lds_ldv<v2u>(aA + 896); mfma_acc(acc[5], a27, b26); SCHED();
    const v2u r14h = lds_ldv<v2u>(aA + 904); const v4u r14 = {r14l.x, r14l.y, r14h.x, r14h.y}; mfma_acc(acc[6], a28, b26); SCHED();
    const v2u r22l = lds_ldv<v2u>(aA + 1408); const v2u r22h = lds_ldv<v2u>(aA + 1416); const v4u r22 = {r22l.x, r22l.y, r22h.x, r22h.y}; mfma_acc(acc[7], a29, b26); SCHED();
    const v2u r30l = lds_ldv<v2u>(aA + 1920); mfma_acc(acc[1], r5, b30); SCHED();
    const v2u r30h = lds_ldv<v2u>(aA + 1928); const v4u r30 = {r30l.x, r30l.y, r30h.x, r30h.y}; mfma_acc(acc[2], r13, b30); SCHED();
    const v2u r38l = lds_ldv<v2u>(aA + 2432); const v2u r38h = lds_ldv<v2u>(aA + 2440); const v4u r38 = {r38l.x, r38l.y, r38h.x, r38h.y}; mfma_acc(acc[3], r21, b30); SCHED();
    const v2u a32l = lds_ldv<v2u>(aA + 2944); mfma_acc(acc[4], r29, b30); SCHED();
    const v2u a32h = lds_ldv<v2u>(aA + 2952); const v4u a32 = {a32l.x, a32l.y, a32h.x, a32h.y}; mfma_acc(acc[5], r37, b30); SCHED();
    const v2u a33l = lds_ldv<v2u>(aA + 3456); const v2u a33h = lds_ldv<v2u>(aA + 3464); const v4u a33 = {a33l.x, a33l.y, a33h.x, a33h.y}; mfma_acc(acc[6], a27, b30); SCHED();
    const v2u a34l = lds_ldv<v2u>(aA + 3968); mfma_acc(acc[7], a28, b30); SCHED();
    const v2u a34h = lds_ldv<v2u>(aA + 3976); const v4u a34 = {a34l.x, a34l.y, a34h.x, a34h.y}; mfma_acc(acc[0], r6, b31); SCHED();
    const v4u b35 = lds_ldv<v4u>(aB + 576); mfma_acc(acc[1], r14, b31); SCHED();
    mfma_acc(acc[2], r22, b31); SCHED();
    const v4u b36 = lds_ldv<v4u>(aB + 0); mfma_acc(acc[3], r30, b31); SCHED();
    const v2u r7l = lds_ldv<v2u>(aA + 448); const v2u r7h = lds_ldv<v2u>(aA + 456); const v4u r7 = {r7l.x, r7l.y, r7h.x, r7h.y}; mfma_acc(acc[4], r38, b31); SCHED();
    const v2u r15l = lds_ldv<v2u>(aA + 960); mfma_acc(acc[5], a32, b31); SCHED();
    const v2u r15h = lds_ldv<v2u>(aA + 968); const v4u r15 = {r15l.x, r15l.y, r15h.x, r15h.y}; mfma_acc(acc[6], a33, b31); SCHED();
    const v2u r23l = lds_ldv<v2u>(aA + 1472); const v2u r23h = lds_ldv<v2u>(aA + 1480); const v4u r23 = {r23l.x, r23l.y, r23h.x, r23h.y}; mfma_acc(acc[7], a34, b31); SCHED();
    const v2u r31l = lds_ldv<v2u>(aA + 1984); mfma_acc(acc[1], r6, b35); SCHED();
    const v2u r31h = lds_ldv<v2u>(aA + 1992); const v4u r31 = {r31l.x, r31l.y, r31h.x, r31h.y}; mfma_acc(acc[2], r14, b35); SCHED();
    const v2u r39l = lds_ldv<v2u>(aA + 2496); const v2u r39h = lds_ldv<v2u>(aA + 2504); const v4u r39 = {r39l.x, r39l.y, r39h.x, r39h.y}; mfma_acc(acc[3], r22, b35); SCHED();
    const v2u a37l = lds_ldv<v2u>(aA + 3008); mfma_acc(acc[4], r30, b35); SCHED();
    const v2u a37h = lds_ldv<v2u>(aA + 3016); const v4u a37 = {a37l.x, a37l.y, a37h.x, a37h.y}; pk = lds_ldv<u32>(C_STG + 4); mfma_acc(acc[5], r38, b35); SCHED();
    const v2u a38l = lds_ldv<v2u>(aA + 3520); const v2u a38h = lds_ldv<v2u>(aA + 3528); const v4u a38 = {a38l.x, a38l.y, a38h.x, a38h.y}; mfma_acc(acc[6], a32, b35); SCHED();
    const v2u a39l = lds_ldv<v2u>(aA + 4032); mfma_acc(acc[7], a33, b35); SCHED();
    const v2u a39h = lds_ldv<v2u>(aA + 4040); const v4u a39 = {a39l.x, a39l.y, a39h.x, a39h.y}; mfma_acc(acc[0], r7, b36); SCHED();
    const v4u b40 = lds_ldv<v4u>(aB + 512); mfma_acc(acc[1], r15, b36); SCHED();
    mfma_acc(acc[2], r23, b36); SCHED();
    need(1); const v4u b41 = lds_ldv<v4u>(aB + 1472); mfma_acc(acc[3], r31, b36); SCHED();
    mfma_acc(acc[4], r39, b36); SCHED();
    mfma_acc(acc[5], a37, b36); SCHED();
    mfma_acc(acc[6], a38, b36); SCHED();
    mfma_acc(acc[7], a39, b36); SCHED();
    const v4u b42 = lds_ldv<v4u>(aB + 1984); mfma_acc(acc[1], r7, b40); SCHED();
    mfma_acc(acc[2], r15, b40); SCHED();
    mfma_acc(acc[3], r23, b40); SCHED();
    mfma_acc(acc[4], r31, b40); SCHED();
    const v4u b43 = lds_ldv<v4u>(aB + 1408); mfma_acc(acc[5], r39, b40); SCHED();
    mfma_acc(acc[6], a37, b40); SCHED();
    mfma_acc(acc[7], a38, b40); SCHED();
    mfma_acc(acc[2], r0, b41); SCHED();
    const v4u b44 = lds_ldv<v4u>(aB + 1920); fin<0, 0>(); mfma_acc(acc[2], AD, b41); SCHED();
    fin<0, 1>(); mfma_acc(acc[3], r8, b41); SCHED();
    mfma_acc(acc[4], r16, b41); SCHED();
    const v4u b45 = lds_ldv<v4u>(aB + 1344); mfma_acc(acc[5], r24, b41); SCHED();
    mfma_acc(acc[3], r0, b42); SCHED();
    mfma_acc(acc[3], AD, b42); SCHED();
    mfma_acc(acc[4], r8, b42); SCHED();
    const v4u b46 = lds_ldv<v4u>(aB + 1856); mfma_acc(acc[5], r16, b42); SCHED();
    mfma_acc(acc[2], r1, b43); SCHED();
    mfma_acc(acc[3], r9, b43); SCHED();
    const v4u b47 = lds_ldv<v4u>(aB + 1280); mfma_acc(acc[4], r17, b43); SCHED();
    mfma_acc(acc[5], r25, b43); SCHED();
    mfma_acc(acc[3], r1, b44); SCHED();
    mfma_acc(acc[4], r9, b44); SCHED();
    const v4u b48 = lds_ldv<v4u>(aB + 1792); mfma_acc(acc[5], r17, b44); SCHED();
    mfma_acc(acc[2], r2, b45); SCHED();
    mfma_acc(acc[3], r10, b45); SCHED();
    const v4u b49 = lds_ldv<v4u>(aB + 1216); mfma_acc(acc[4], r18, b45); SCHED();
    mfma_acc(acc[5], r26, b45); SCHED();
    mfma_acc(acc[3], r2, b46); SCHED();
    mfma_acc(acc[4], r10, b46); SCHED();
    const v4u b50 = lds_ldv<v4u>(aB + 1728); mfma_acc(acc[5], r18, b46); SCHED();
    mfma_acc(acc[2], r3, b47); SCHED();
    mfma_acc(acc[3], r11, b47); SCHED();
    const v4u b51 = lds_ldv<v4u>(aB + 1152); mfma_acc(acc[4], r19, b47); SCHED();
    mfma_acc(acc[5], r27, b47); SCHED();
    mfma_acc(acc[3], r3, b48); SCHED();
    mfma_acc(acc[4], r11, b48); SCHED();
    const v4u b52 = lds_ldv<v4u>(aB + 1664); mfma_acc(acc[5], r19, b48); SCHED();
    mfma_acc(acc[2], r4, b49); SCHED();
    mfma_acc(acc[3], r12, b49); SCHED();
    const v4u b53 = lds_ldv<v4u>(aB + 1088); mfma_acc(acc[4], r20, b49); SCHED();
    mfma_acc(acc[5], r28, b49); SCHED();
    mfma_acc(acc[3], r4, b50); SCHED();
    mfma_acc(acc[4], r12, b50); SCHED();
    const v4u b54 = lds_ldv<v4u>(aB + 1600); mfma_acc(acc[5], r20, b50); SCHED();
    mfma_acc(acc[2], r5, b51); SCHED();
    mfma_acc(acc[3], r13, b51); SCHED();
    const v4u b55 = lds_ldv<v4u>(aB + 1024); mfma_acc(acc[4], r21, b51); SCHED();
    pk = lds_ldv<u32>(C_STG + 8); mfma_acc(acc[5], r29, b51); SCHED();
    mfma_acc(acc[3], r5, b52); SCHED();
    mfma_acc(acc[4], r13, b52); SCHED();
    const v4u b56 = lds_ldv<v4u>(aB + 1536); mfma_acc(acc[5], r21, b52); SCHED();
    mfma_acc(acc[2], r6, b53); SCHED();
    mfma_acc(acc[3], r14, b53); SCHED();
    need(2); const v4u b57 = lds_ldv<v4u>(aB + 2496); mfma_acc(acc[4], r22, b53); SCHED();
    mfma_acc(acc[5], r30, b53); SCHED();
    mfma_acc(acc[3], r6, b54); SCHED();
    const v4u b58 = lds_ldv<v4u>(aB + 3008); mfma_acc(acc[4], r14, b54); SCHED();
    mfma_acc(acc[5], r22, b54); SCHED();
    const v4u b59 = lds_ldv<v4u>(aB + 2432); mfma_acc(acc[2], r7, b55); SCHED();
    mfma_acc(acc[3], r15, b55); SCHED();
    const v4u b60 = lds_ldv<v4u>(aB + 2944); mfma_acc(acc[4], r23, b55); SCHED();
    const v4u b61 = lds_ldv<v4u>(aB + 2368); mfma_acc(acc[5], r31, b55); SCHED();
    mfma_acc(acc[3], r7, b56); SCHED();
    const v4u b62 = lds_ldv<v4u>(aB + 2880); mfma_acc(acc[4], r15, b56); SCHED();
    const v4u b63 = lds_ldv<v4u>(aB + 2304); mfma_acc(acc[5], r23, b56); SCHED();
    mfma_acc(acc[4], r0, b57); SCHED();
    const v4u b64 = lds_ldv<v4u>(aB + 2816); mfma_acc(acc[4], AD, b57); SCHED();
    const v4u b65 = lds_ldv<v4u>(aB + 2240); mfma_acc(acc[5], r8, b57); SCHED();
    mfma_acc(acc[5], r0, b58); SCHED();
    const v4u b66 = lds_ldv<v4u>(aB + 2752); mfma_acc(acc[5], AD, b58); SCHED();
    const v4u b67 = lds_ldv<v4u>(aB + 2176); fin<1, 0>(); mfma_acc(acc[4], r1, b59); SCHED();
    fin<1, 1>(); mfma_acc(acc[5], r9, b59); SCHED();
    const v4u b68 = lds_ldv<v4u>(aB + 2688); mfma_acc(acc[5], r1, b60); SCHED();
    const v4u b69 = lds_ldv<v4u>(aB + 2112); mfma_acc(acc[4], r2, b61); SCHED();
    mfma_acc(acc[5], r10, b61); SCHED();
    const v4u b70 = lds_ldv<v4u>(aB + 2624); mfma_acc(acc[5], r2, b62); SCHED();
    const v4u b71 = lds_ldv<v4u>(aB + 2048); mfma_acc(acc[4], r3, b63); SCHED();
    mfma_acc(acc[5], r11, b63); SCHED();
    const v4u b72 = lds_ldv<v4u>(aB + 2560); mfma_acc(acc[5], r3, b64); SCHED();
    const v4u b73 = lds_ldv<v4u>(aB + 1472); mfma_acc(acc[4], r4, b65); SCHED();
    const v2u a74l = lds_ldv<v2u>(aA + 2560); const v2u a74h = lds_ldv<v2u>(aA + 2568); const v4u a74 = {a74l.x, a74l.y, a74h.x, a74h.y}; mfma_acc(acc[5], r12, b65); SCHED();
    const v4u b75 = lds_ldv<v4u>(aB + 1984); mfma_acc(acc[5], r4, b66); SCHED();
    mfma_acc(acc[4], r5, b67); SCHED();
    const v4u b76 = lds_ldv<v4u>(aB + 1408); mfma_acc(acc[5], r13, b67); SCHED();
    const v2u a77l = lds_ldv<v2u>(aA + 2624); mfma_acc(acc[5], r5, b68); SCHED();
    const v2u a77h = lds_ldv<v2u>(aA + 2632); const v4u a77 = {a77l.x, a77l.y, a77h.x, a77h.y}; mfma_acc(acc[4], r6, b69); SCHED();
    const v4u b78 = lds_ldv<v4u>(aB + 1920); mfma_acc(acc[5], r14, b69); SCHED();
    const v4u b79 = lds_ldv<v4u>(aB + 1344); mfma_acc(acc[5], r6, b70); SCHED();
    const v2u a80l = lds_ldv<v2u>(aA + 2688); mfma_acc(acc[4], r7, b71); SCHED();
    const v2u a80h = lds_ldv<v2u>(aA + 2696); const v4u a80 = {a80l.x, a80l.y, a80h.x, a80h.y}; const v4u b81 = lds_ldv<v4u>(aB + 1856); mfma_acc(acc[5], r15, b71); SCHED();
    mfma_acc(acc[5], r7, b72); SCHED();
    const v4u b82 = lds_ldv<v4u>(aB + 1280); mfma_acc(acc[6], r32, b73); SCHED();
    const v2u a83l = lds_ldv<v2u>(aA + 2752); const v2u a83h = lds_ldv<v2u>(aA + 2760); const v4u a83 = {a83l.x, a83l.y, a83h.x, a83h.y}; mfma_acc(acc[7], a74, b73); SCHED();
    const v4u b84 = lds_ldv<v4u>(aB + 1792); mfma_acc(acc[6], r24, b75); SCHED();
    mfma_acc(acc[7], r32, b75); SCHED();
    const v4u b85 = lds_ldv<v4u>(aB + 1216); mfma_acc(acc[6], r33, b76); SCHED();
    const v2u a86l = lds_ldv<v2u>(aA + 2816); mfma_acc(acc[7], a77, b76); SCHED();
    const v2u a86h = lds_ldv<v2u>(aA + 2824); const v4u a86 = {a86l.x, a86l.y, a86h.x, a86h.y}; mfma_acc(acc[6], r25, b78); SCHED();
    const v4u b87 = lds_ldv<v4u>(aB + 1728); fin<2, 0>(); mfma_acc(acc[7], r33, b78); SCHED();
    const v4u b88 = lds_ldv<v4u>(aB + 1152); fin<2, 1>(); mfma_acc(acc[6], r34, b79); SCHED();
    const v2u a89l = lds_ldv<v2u>(aA + 2880); mfma_acc(acc[7], a80, b79); SCHED();
    const v2u a89h = lds_ldv<v2u>(aA + 2888); const v4u a89 = {a89l.x, a89l.y, a89h.x, a89h.y}; const v4u b90 = lds_ldv<v4u>(aB + 1664); mfma_acc(acc[6], r26, b81); SCHED();
    mfma_acc(acc[7], r34, b81); SCHED();
    const v4u b91 = lds_ldv<v4u>(aB + 1088); mfma_acc(acc[6], r35, b82); SCHED();
    const v2u a92l = lds_ldv<v2u>(aA + 2944); const v2u a92h = lds_ldv<v2u>(aA + 2952); const v4u a92 = {a92l.x, a92l.y, a92h.x, a92h.y}; mfma_acc(acc[7], a83, b82); SCHED();
    const v4u b93 = lds_ldv<v4u>(aB + 1600); mfma_acc(acc[6], r27, b84); SCHED();
    mfma_acc(acc[7], r35, b84); SCHED();
    const v4u b94 = lds_ldv<v4u>(aB + 1024); mfma_acc(acc[6], r36, b85); SCHED();
    const v2u a95l = lds_ldv<v2u>(aA + 3008); mfma_acc(acc[7], a86, b85); SCHED();
    const v2u a95h = lds_ldv<v2u>(aA + 3016); const v4u a95 = {a95l.x, a95l.y, a95h.x, a95h.y}; mfma_acc(acc[6], r28, b87); SCHED();
    const v4u b96 = lds_ldv<v4u>(aB + 1536); mfma_acc(acc[7], r36, b87); SCHED();
    const v4u b97 = lds_ldv<v4u>(aB + 2496); mfma_acc(acc[6], r37, b88); SCHED();
    mfma_acc(acc[7], a89, b88); SCHED();
    const v4u b98 = lds_ldv<v4u>(aB + 3008); mfma_acc(acc[6], r29, b90); SCHED();
    mfma_acc(acc[7], r37, b90); SCHED();
    const v4u b99 = lds_ldv<v4u>(aB + 2432); mfma_acc(acc[6], r38, b91); SCHED();
    mfma_acc(acc[7], a92, b91); SCHED();
    const v4u b100 = lds_ldv<v4u>(aB + 2944); mfma_acc(acc[6], r30, b93); SCHED();
    mfma_acc(acc[7], r38, b93); SCHED();
    const v4u b101 = lds_ldv<v4u>(aB + 2368); mfma_acc(acc[6], r39, b94); SCHED();
    mfma_acc(acc[7], a95, b94); SCHED();
    const v4u b102 = lds_ldv<v4u>(aB + 2880); mfma_acc(acc[6], r31, b96); SCHED();
    mfma_acc(acc[7], r39, b96); SCHED();
    const v4u b103 = lds_ldv<v4u>(aB + 2304); mfma_acc(acc[6], r16, b97); SCHED();
    mfma_acc(acc[7], r24, b97); SCHED();
    const v4u b104 = lds_ldv<v4u>(aB + 2816); mfma_acc(acc[6], r8, b98); SCHED();
    mfma_acc(acc[7], r16, b98); SCHED();
    const v4u b105 = lds_ldv<v4u>(aB + 2240); mfma_acc(acc[6], r17, b99); SCHED();
    mfma_acc(acc[7], r25, b99); SCHED();
    const v4u b106 = lds_ldv<v4u>(aB + 2752); mfma_acc(acc[6], r9, b100); SCHED();
    mfma_acc(acc[7], r17, b100); SCHED();
    const v4u b107 = lds_ldv<v4u>(aB + 2176); mfma_acc(acc[6], r18, b101); SCHED();
    mfma_acc(acc[7], r26, b101); SCHED();
    const v4u b108 = lds_ldv<v4u>(aB + 2688); mfma_acc(acc[6], r10, b102); SCHED();
    mfma_acc(acc[7], r18, b102); SCHED();
    const v4u b109 = lds_ldv<v4u>(aB + 2112); mfma_acc(acc[6], r19, b103); SCHED();
    mfma_acc(acc[7], r27, b103); SCHED();
    const v4u b110 = lds_ldv<v4u>(aB + 2624); pk = lds_ldv<u32>(C_STG + 12); mfma_acc(acc[6], r11, b104); SCHED();
    mfma_acc(acc[7], r19, b104); SCHED();
    const v4u b111 = lds_ldv<v4u>(aB + 2048); mfma_acc(acc[6], r20, b105); SCHED();
    mfma_acc(acc[7], r28, b105); SCHED();
    const v4u b112 = lds_ldv<v4u>(aB + 2560); mfma_acc(acc[6], r12, b106); SCHED();
    mfma_acc(acc[7], r20, b106); SCHED();
    need(3); const v4u b113 = lds_ldv<v4u>(aB + 3520); mfma_acc(acc[6], r21, b107); SCHED();
    mfma_acc(acc[7], r29, b107); SCHED();
    mfma_acc(acc[6], r13, b108); SCHED();
    const v4u b114 = lds_ldv<v4u>(aB + 4032); mfma_acc(acc[7], r21, b108); SCHED();
    mfma_acc(acc[6], r22, b109); SCHED();
    const v4u b115 = lds_ldv<v4u>(aB + 3456); mfma_acc(acc[7], r30, b109); SCHED();
    mfma_acc(acc[6], r14, b110); SCHED();
    const v4u b116 = lds_ldv<v4u>(aB + 3968); mfma_acc(acc[7], r22, b110); SCHED();
    const v4u b117 = lds_ldv<v4u>(aB + 3392); mfma_acc(acc[6], r23, b111); SCHED();
    mfma_acc(acc[7], r31, b111); SCHED();
    const v4u b118 = lds_ldv<v4u>(aB + 3904); mfma_acc(acc[6], r15, b112); SCHED();
    const v4u b119 = lds_ldv<v4u>(aB + 3328); mfma_acc(acc[7], r23, b112); SCHED();
    mfma_acc(acc[6], r0, b113); SCHED();
    const v4u b120 = lds_ldv<v4u>(aB + 3840); mfma_acc(acc[6], AD, b113); SCHED();
    const v4u b121 = lds_ldv<v4u>(aB + 3264); mfma_acc(acc[7], r8, b113); SCHED();
    mfma_acc(acc[7], r0, b114); SCHED();
    const v4u b122 = lds_ldv<v4u>(aB + 3776); mfma_acc(acc[7], AD, b114); SCHED();
    const v4u b123 = lds_ldv<v4u>(aB + 3200); mfma_acc(acc[6], r1, b115); SCHED();
    mfma_acc(acc[7], r9, b115); tl[0] = lds_ldv<v4u>(park); SCHED();
    const v4u b124 = lds_ldv<v4u>(aB + 3712); mfma_acc(acc[7], r1, b116); SCHED();
    const v4u b125 = lds_ldv<v4u>(aB + 3136); mfma_acc(acc[6], r2, b117); SCHED();
    mfma_acc(acc[7], r10, b117); tl[1] = lds_ldv<v4u>(park + 1024); SCHED();
    const v4u b126 = lds_ldv<v4u>(aB + 3648); mfma_acc(acc[7], r2, b118); SCHED();
    const v4u b127 = lds_ldv<v4u>(aB + 3072); mfma_acc(acc[6], r3, b119); SCHED();
    mfma_acc(acc[7], r11, b119); SCHED();
    const v4u b128 = lds_ldv<v4u>(aB + 3584); mfma_acc(acc[7], r3, b120); SCHED();
    mfma_acc(acc[6], r4, b121); SCHED();
    mfma_acc(acc[7], r12, b121); tl[2] = lds_ldv<v4u>(park + 2048); SCHED();
    mfma_acc(acc[7], r4, b122); SCHED();
    mfma_acc(acc[6], r5, b123); tl[3] = lds_ldv<v4u>(park + 3072); SCHED();
    mfma_acc(acc[7], r13, b123); SCHED();
    mfma_acc(acc[7], r5, b124); SCHED();
    mfma_acc(acc[6], r6, b125); SCHED();
    mfma_acc(acc[7], r14, b125); SCHED();
    mfma_acc(acc[7], r6, b126); SCHED();
    mfma_acc(acc[6], r7, b127); SCHED();
    mfma_acc(acc[7], r15, b127); SCHED();
    mfma_acc(acc[7], r7, b128); SCHED();
    asm volatile("s_nop 7" : "+v"(acc[6]), "+v"(acc[7]));
    fin<3, 0>(); fin<3, 1>();
  }
  __device__ __forceinline__ void setup(int w, int lane, u32 Dbits) {
    const int M = lane & 15, q4 = lane >> 4;
    const u32 ub = L_UB + w * UB_CH, hb = L_HC + w * HC_CH;
    aA = hb + ((M + 1) & 3) * HC_COPY + ((M + 1) >> 2) * 8 + q4 * 16;
    const int jd = 15 - M - 8 * q4;
#pragma unroll
    for (int i = 0; i < 4; ++i) AD[i] = jd == 2 * i ? Dbits : jd == 2 * i + 1 ? (Dbits << 16) : 0u;
    aB = ub + 512 - 7 * 64 + (2 * M + 1 - q4) * 16;
    osw = L_OS + w * OS_CH + (16 * M + 4 * q4) * 2;
    flg = C_OUT + w * 4;
    asm volatile("" : "+v"(aB), "+v"(aA), "+v"(flg), "+v"(osw), "+v"(AD));
  }
};

__device__ __forceinline__ u32 get_tag() {
  u32 tag = 0;
  SPIN(SPIN_L) {
    const v2u f = lds_ldv<v2u>(C_TAG);
    tag = rfl(f.y);
    if (rfl(f.x)) break;
    __builtin_amdgcn_s_sleep(1);
  }
  return tag;
}

__device__ __forceinline__ v4u chk_consts(int lane) {
  const u32 m = (u32)(lane & 15) * 0x1E3A2B46u;
  u32 k0 = 0x9E37B5C3u + m, k1 = 0xC2B3AE3Du ^ m, k2 = 0x85EBF4A7u - m, k3 = 0xD35B6F19u + 3u * m;
  asm volatile("" : "+v"(k0), "+v"(k1), "+v"(k2), "+v"(k3));
  return (v4u){k0, k1, k2, k3};
}
__device__ __forceinline__ u32 chk_piece(v4u d, v4u k, u32 a) { return a + d.x * k.x + d.y * k.y + d.z * k.z + d.w * k.w; }
#define ROWSUM(ctl) "v_add_u32_dpp %0, %0, %0 " ctl " row_mask:0xf bank_mask:0xf\n\tv_add_u32_dpp %1, %1, %1 " ctl " row_mask:0xf bank_mask:0xf\n\t" \
                    "v_add_u32_dpp %2, %2, %2 " ctl " row_mask:0xf bank_mask:0xf\n\tv_add_u32_dpp %3, %3, %3 " ctl " row_mask:0xf bank_mask:0xf\n\t"
template <bool OCT> __device__ __forceinline__ u32 chk_make(const v4u* d, v4u k, int lane) {
  u32 h[4];
#pragma unroll
  for (int i = 0; i < 4; ++i) h[i] = chk_piece(d[i], k, 0u);
  if constexpr (OCT) asm("s_nop 2\n\t" ROWSUM("quad_perm:[1,0,3,2]") ROWSUM("quad_perm:[2,3,0,1]") ROWSUM("row_half_mirror") : "+v"(h[0]), "+v"(h[1]), "+v"(h[2]), "+v"(h[3]));
  else asm("s_nop 2\n\t" ROWSUM("row_ror:1") ROWSUM("row_ror:2") ROWSUM("row_ror:4") ROWSUM("row_ror:8") : "+v"(h[0]), "+v"(h[1]), "+v"(h[2]), "+v"(h[3]));
  const u32 lo = (lane & 1) ? h[1] : h[0], hi = (lane & 1) ? h[3] : h[2];
  return (lane & 2) ? hi : lo;
}
#undef ROWSUM
template <bool OCT> __device__ __forceinline__ bool chk_bad(const v4u* d, v4u k, u32 neg) {
  u32 a = chk_piece(d[1], k, chk_piece(d[0], k, neg)) + chk_piece(d[3], k, chk_piece(d[2], k, 0u));
  if constexpr (OCT) { a += dppu<0xB1, 0xf>(a); a += dppu<0x4E, 0xf>(a); a += dppu<0x141, 0xf>(a); }
  else { a += dppu<0x121, 0xf>(a); a += dppu<0x122, 0xf>(a); a += dppu<0x124, 0xf>(a); a += dppu<0x128, 0xf>(a); }
  return a != 0u;
}

__device__ __forceinline__ u32 now() { return (u32)__builtin_amdgcn_s_memrealtime(); }
__device__ __forceinline__ void hold(u32 t0, u32 dt) {
  SPIN(4096) { if (now() - t0 >= dt) break; __builtin_amdgcn_s_sleep(1); }
}

struct EpiPrep {
  v4u kc, a, b;
  __device__ __forceinline__ void make(u32 row0, u32 tag, int lane) {
    const u32 r0 = (lane & 4) * 2 + (lane & 3);
    kc = chk_consts(lane & 7);
    a.x = WS_YX + (row0 >> 4) * YSTR + (row0 & 8) * 2048 + lane * 16;
    a.y = (lane & 4) ? OOB : WS_YC + ((row0 & 8) * 16 + (lane & 3) * 8 + (lane >> 3)) * 2048 + ((row0 >> 4) & 7) * 32 +
                             (row0 >= 1536 ? 1024 + ((row0 >> 7) & 3) * 256 : (row0 >> 9) * 256 + ((row0 >> 7) & 3) * 4);
    a.z = (lane & 4) ? 0u : tag * TAGK;
    a.w = L_XT + r0 * XT_P + (lane >> 3) * 8;
    b.x = L_RN + r0 * 8;
    b.y = (row0 & ~15u) + (lane >> 4);
    b.z = b.y * 2048 + (row0 & 8) * 128 + (lane & 15) * 16;
    b.w = L_XT + (lane >> 4) * XT_P + (lane & 15) * 16;
    asm volatile("" : "+v"(a), "+v"(b));
  }
};
template <bool HELPER> __device__ __forceinline__ void epilogue(rsrc_t rws, rsrc_t ro, const EpiPrep& e, u32 row0, int lane) {
  const u32 yo = e.a.x, co = e.a.y, tm = e.a.z;
  const v4u kc = e.kc;
  SPIN(4) {
    u32 qd = 0;
    if (lane == 0) qd = __hip_atomic_fetch_add((LAS u32*)(size_t)C_TKT, 1u, __ATOMIC_RELAXED, __HIP_MEMORY_SCOPE_WORKGROUP);
    const u32 yok = lds_ldv<u32>(C_YOK);
    qd = rfl(qd);
    if (qd >= 4) break;
    if (!rfl(yok)) lds_wait_nz(C_YOK);
    MEMFENCE();
    v4u y[4];
#pragma unroll
    for (int i = 0; i < 4; ++i) y[i] = __builtin_amdgcn_raw_buffer_load_b128(rws, yo + i * 1024, qd * 4096, SC1);
    u32 c = __builtin_amdgcn_raw_buffer_load_b32(rws, co, qd * 65536, SC1);
    SCHED();
    const u32 xa = e.a.w + qd * 256;
    float xf[4][8], xn[4][8];
    const v4u ks4 = lds_ldv<v4u>(C_SEL);
    {
      const f32x2 s0 = lds_ld<f32x2>(e.b.x), s1 = lds_ld<f32x2>(e.b.x + 32);
      v2u xw[8];
#pragma unroll
      for (int i = 0; i < 8; ++i) xw[i] = lds_ld<v2u>(xa + (i >> 1) * 64 + (i & 1) * 4 * XT_P);
#pragma unroll
      for (int i = 0; i < 4; ++i) {
        const v2u a = xw[2 * i], b = xw[2 * i + 1];
        xf[i][0] = lo2f(a.x); xf[i][1] = hi2f(a.x); xf[i][2] = lo2f(a.y); xf[i][3] = hi2f(a.y);
        xf[i][4] = lo2f(b.x); xf[i][5] = hi2f(b.x); xf[i][6] = lo2f(b.y); xf[i][7] = hi2f(b.y);
#pragma unroll
        for (int e = 0; e < 8; ++e) xn[i][e] = e < 4 ? __builtin_fmaf(xf[i][e], s0.x, s0.y) : __builtin_fmaf(xf[i][e], s1.x, s1.y);
      }
#pragma unroll
      for (int i = 0; i < 4; ++i)
#pragma unroll
        for (int e = 0; e < 8; ++e) asm volatile("" : "+v"(xf[i][e]), "+v"(xn[i][e]));
    }
    const u32 i0 = 2047u - (rfl(ks4.y) & 0x7FFu), i1 = 2047u - (rfl(ks4.z) & 0x7FFu);
    u32 oo[4];
#pragma unroll
    for (int i = 0; i < 4; ++i) {
      oo[i] = ((e.b.y == i0 - 4 * i) | (e.b.y == i1 - 4 * i)) ? OOB : e.b.z;
      asm volatile("" : "+v"(oo[i]));
    }
    u32 ta = e.b.w + qd * 256;
    asm volatile("" : "+v"(ta));
    SCHED();
    SPIN(SPIN_G) {
      const bool bad = chk_bad<true>(y, kc, tm - c);
      if (__builtin_amdgcn_ballot_w64(bad) == 0ull) break;
      MEMFENCE();
      if (bad) {
#pragma unroll
        for (int i = 0; i < 4; ++i) y[i] = __builtin_amdgcn_raw_buffer_load_b128(rws, yo + i * 1024, qd * 4096, SC1);
        c = __builtin_amdgcn_raw_buffer_load_b32(rws, co, qd * 65536, SC1);
      }
    }
#pragma unroll
    for (int i = 0; i < 4; ++i) {
      u32 o[4];
#pragma unroll
      for (int d = 0; d < 4; ++d) {
        const u32 yy = y[i][d];
        if constexpr (HELPER) o[d] = pk2(fma1(xn[i][2 * d], lo2f(yy), xf[i][2 * d]), fma1(xn[i][2 * d + 1], hi2f(yy), xf[i][2 * d + 1]));
        else o[d] = pk2(__builtin_fmaf(xn[i][2 * d], lo2f(yy), xf[i][2 * d]), __builtin_fmaf(xn[i][2 * d + 1], hi2f(yy), xf[i][2 * d + 1]));
      }
      lds_st<v2u>(xa + i * 64, (v2u){o[0], o[1]});
      lds_st<v2u>(xa + i * 64 + 4 * XT_P, (v2u){o[2], o[3]});
    }
    MEMFENCE();
#pragma unroll
    for (int i = 0; i < 4; ++i) __builtin_amdgcn_raw_buffer_store_b128(lds_ld<v4u>(ta + i * 4 * XT_P), ro, oo[i], qd * 256 + i * 4 * 2048, SC1);
  }
}

typedef u32 v16u __attribute__((ext_vector_type(16)));
typedef u32 v8u __attribute__((ext_vector_type(8)));
#define ENDW() do { asm volatile("s_endpgm" : "=v"(Q[0]), "=v"(Q[1]), "=v"(Q[2]), "=v"(Q[3]), "=v"(Q[4]), "=v"(Q[5]), "=v"(Q[6]), "=v"(Q[7]), "=v"(Q[8]), "=v"(Q[9]) :: "memory"); } while (0)

__global__ void __launch_bounds__(512, 2) mega(Params p_unused) {
  extern __shared__ __attribute__((aligned(16))) char smem[];
  u64 pc0;
  v16u ka;
  v8u kb;
  asm volatile("s_load_dwordx16 %1, %3, 0x0\n\ts_load_dwordx8 %2, %3, 0x40\n\ts_getpc_b64 %0"
               : "=&s"(pc0), "=&s"(ka), "=&s"(kb) : "s"(__builtin_amdgcn_kernarg_segment_ptr()));
  const int tid = threadIdx.x;
  int wave = __builtin_amdgcn_readfirstlane(tid >> 6);
  const int lane = tid & 63;
  const int b = blockIdx.x, xcd = b & 7, rk = b >> 3;
  const u32 codepf = ld4<0>(mkrs((const void*)(pc0 & ~255ull), CODE_KB * 1024), (rk & 15) * 3072 + wave * 384 + lane * 128 + (lane >= 3 ? OOB : 0u));
  SCHED();
  const int L = xcd * 32 + rk;
  const int jw = wave & 3, cg = L >> 4, rb = L & 15, ch = 4 * L + jw;
  const u32 row0 = (u32)((((L >> 1) & 3) * 32 + (L >> 3)) * 16 + (L & 1) * 8);
  const bool cw = wave < 4;
  const u32 fbase = cw ? (u32)((jw * 512 + rb * 32) * 2048 + cg * 128) : (row0 + 2 * jw) * 2048;
  u32 lo16 = (lane & (cw ? 7 : 31)) * 16;
  u32 toff = fbase + ((lane >> (cw ? 3 : 5)) << (cw ? 13 : 11)) + lo16;
  u32 toff2 = cw ? toff : lo16;
  u32 so0 = cw ? 0u : (u32)(L & 1) * 1024u, so1 = so0 + (cw ? 2048u : 512u), so2 = cw ? 4096u : so0 ^ 1024u, so3 = so2 + (cw ? 2048u : 512u);
  asm volatile("" : "+v"(toff), "+v"(toff2), "+s"(so0), "+s"(so1), "+s"(so2), "+s"(so3));
  asm volatile("s_waitcnt lgkmcnt(0)" : "+s"(ka), "+s"(kb) :: "memory");
  u64 tk;
  asm volatile("s_memrealtime %0" : "=s"(tk));
  Params p;
#define KP(v, i) (((u64)v[2 * (i) + 1] << 32) | v[2 * (i)])
  p.q = (const u16*)KP(ka, 0); p.k = (const u16*)KP(ka, 1); p.v = (const u16*)KP(ka, 2); p.shk = (const u16*)KP(ka, 3);
  p.ssmk = (const u16*)KP(ka, 4); p.ssmD = (const u16*)KP(ka, 5); p.wr = (const u16*)KP(ka, 6); p.wg = (const u16*)KP(ka, 7);
  p.wu = (const u16*)KP(kb, 0); p.wd = (const u16*)KP(kb, 1); p.out = (u16*)KP(kb, 2); p.ws = (char*)KP(kb, 3);
#undef KP
  v4u Q[10];
  auto small_loads = [&]() {
    Q[8] = ld16<NT>(mkrs(p.k, S * D * 2), lane < 24 ? toff - (lane >> 3) * 3 * 2048 - 3 * 2048 : OOB);
    Q[9] = ld16<0>(mkrs(p.shk, D * 8), lane < 32 ? cg * 512 + lane * 16 : OOB);
    SCHED();
  };
  if ((u32)(wave - 1) >= 3u) {
    if (!cw) __builtin_amdgcn_s_sleep(4);
    const rsrc_t r1 = mkrs(cw ? p.k : p.q, S * D * 2), r2 = mkrs(cw ? p.v : p.wr, S * D * 2);
    Q[0] = __builtin_amdgcn_raw_buffer_load_b128(r1, toff, so0, NT);
    Q[1] = __builtin_amdgcn_raw_buffer_load_b128(r1, toff, so1, NT);
    Q[2] = __builtin_amdgcn_raw_buffer_load_b128(r1, toff, so2, NT);
    Q[3] = __builtin_amdgcn_raw_buffer_load_b128(r1, toff, so3, NT);
    if (cw) {
      Q[4] = __builtin_amdgcn_raw_buffer_load_b128(r2, toff2, so0, NT);
      Q[5] = __builtin_amdgcn_raw_buffer_load_b128(r2, toff2, so1, NT);
      Q[6] = __builtin_amdgcn_raw_buffer_load_b128(r2, toff2, so2, NT);
      Q[7] = __builtin_amdgcn_raw_buffer_load_b128(r2, toff2, so3, NT);
      SCHED();
      small_loads();
    } else {
      Q[4] = __builtin_amdgcn_raw_buffer_load_b128(r2, toff2, so0, 0);
      Q[5] = __builtin_amdgcn_raw_buffer_load_b128(r2, toff2, so1, 0);
      Q[6] = __builtin_amdgcn_raw_buffer_load_b128(r2, toff2, so2, 0);
      Q[7] = __builtin_amdgcn_raw_buffer_load_b128(r2, toff2, so3, 0);
      Q[8] = __builtin_amdgcn_raw_buffer_load_b128(r1, toff ^ (8 * 2048), so0, NT);
      Q[9] = __builtin_amdgcn_raw_buffer_load_b128(r1, toff ^ (8 * 2048), so1, NT);
    }
    SCHED();
  }
  asm volatile("" : "+s"(wave));
  lds_st<u32>(L_CTL + lane * 4, 0u);
  asm volatile("s_waitcnt lgkmcnt(0)" : "+s"(tk) :: "memory");
  u32 t0 = (u32)tk - 25u;
  const bool ffn = rk < 28;
  const int j = xcd * 28 + rk;
  const rsrc_t rq = mkrs(p.q, S * D * 2);
  __builtin_amdgcn_s_barrier();
  SCHED();
  const rsrc_t rws = mkrs(p.ws, WS_END);
  u32 epoch = 0;
  if (wave == 7) {
    epoch = *(const CAS u32*)(p.ws + WS_EPOCH);
    asm volatile("" : "+s"(epoch));
    lds_stv<v2u>(C_TAG, (v2u){1u, epoch + 1u});
  }
  asm volatile("" : "+s"(wave));

  if (wave < 4) {
    const int w = jw;
    const int g = lane & 7, rr = lane >> 3;
    v4u U[15];
    const rsrc_t rk_ = mkrs(p.k, S * D * 2);
    const u32 Dw = *(const CAS u32*)(p.ssmD + (ch & ~1));
    SCHED();
    const u32 hb = L_TS + w * 5120;
    u32 tw = hb + g * 528 + (rr >> 1) * 16 + ((rr & 1) ? 0 : 8), tr_ = hb + (lane >> 5) * 528 + (lane & 31) * 16;
    u32 ko = WS_KX + (cg * 64 + w * 16 + rb) * 4096 + lane * 16;
    asm volatile("" : "+v"(tw), "+v"(tr_), "+v"(ko));
    if (lane < 32) lds_st<v4u>(L_UB + w * UB_CH + lane * 16, (v4u){0, 0, 0, 0});
    const v4u kc = chk_consts(lane);
    const u32 tag = get_tag();
    Conv cv;
    cv.setup(w, lane, (ch & 1) ? (Dw >> 16) : (Dw & 0xFFFFu));
    u32 fo = WS_KVF + (cg * 64 + w * 16 + rb) * 256 + lane * 4;
    asm volatile("" : "+v"(fo));
    cv.park = L_PK + w * 4096 + lane * 16;
    auto park = [&]() {
      EpiPrep e;
      e.make(row0, tag, lane);
      lds_st<v4u>(cv.park, (v4u){L_OS + ((lane >> 2) & 3) * OS_CH + (1536 + w * 128 + (lane >> 4) * 32 + (lane & 3) * 8) * 2,
                            WS_YX + (96 + w * 8 + (lane >> 3)) * YSTR + L * 128 + (lane & 7) * 16, WS_YC + L * 2048 + 1024 + w * 256 + lane * 4, 0u});
      lds_st<v4u>(cv.park + 1024, e.kc);
      lds_st<v4u>(cv.park + 2048, e.a);
      lds_st<v4u>(cv.park + 3072, e.b);
      MEMFENCE();
    };
    if (w == 3) {
      if (ffn) {
        const v4u sent = {SENT, SENT, SENT, SENT};
        st16<SC1>(sent, rws, WS_GU + j * 1024 + lane * 16);
        st16<SC1>(sent, rws, WS_DN + j * 2048 + lane * 16);
        st16<SC1>(sent, rws, WS_DN + j * 2048 + 1024 + lane * 16);
      }
      VMCNT(0);
      lds_stv<u32>(C_CLR, 1u);
    }
    if (w) {
      if (w >= 2) {
        lds_wait_nz(C_T0);
        t0 = rfl(lds_ldv<u32>(C_T0));
      }
      hold(t0, w == 1 ? T_S1 : w == 2 ? T_S2 : T_S3);
#pragma unroll
      for (int i = 0; i < 4; ++i) Q[i] = ld16<NT>(rk_, toff + i * 2048);
      small_loads();
#pragma unroll
      for (int i = 0; i < 4; ++i) Q[4 + i] = ld16<NT>(mkrs(p.v, S * D * 2), toff + i * 2048);
      SCHED();
      park();
    }
#pragma unroll
    for (int i = 0; i < 8; ++i) U[3 + i] = Q[i];
    u32 hl = hb + lane * 16;
    asm volatile("" : "+v"(hl) : "v"(codepf));
    lds_st<v4u>(hl + 4096, Q[9]);
    if (lane < 24) lds_st<v4u>(hb + rr * 1152 + g * 16, Q[8]);
    MEMFENCE();
#pragma unroll
    for (int i = 0; i < 4; ++i) U[11 + i] = lds_ld<v4u>(hb + 4096 + g * 64 + i * 16);
#pragma unroll
    for (int i = 0; i < 3; ++i) lds_st<v4u>(hl + 128 + i * 1152, U[4 + i]);
    MEMFENCE();
#pragma unroll
    for (int i = 0; i < 3; ++i) U[i] = lds_ld<v4u>(hl + i * 1152);
    VMCNT(0);
    SCHED();
#pragma unroll
    for (int c = 0; c < 8; ++c) {
      const u32 wa = U[11 + (c >> 1)][(c & 1) * 2], wb = U[11 + (c >> 1)][(c & 1) * 2 + 1];
      const u32 sel = (c & 1) ? 0x07060302u : 0x05040100u;
      u32 P[6];
      float ks[4], vf[4];
#pragma unroll
      for (int i = 0; i < 6; ++i) P[i] = __builtin_amdgcn_perm(U[i][c >> 1], U[i + 1][c >> 1], sel);
#pragma unroll
      for (int i = 0; i < 4; ++i) { const u32 vx = U[7 + i][c >> 1]; vf[i] = (c & 1) ? hi2f(vx) : lo2f(vx); }
      asm("v_dot2_f32_bf16 %0, %4, %11, 0\n\tv_dot2_f32_bf16 %1, %5, %11, 0\n\tv_dot2_f32_bf16 %2, %6, %11, 0\n\tv_dot2_f32_bf16 %3, %7, %11, 0\n\t"
          "v_dot2_f32_bf16 %0, %6, %10, %0\n\tv_dot2_f32_bf16 %1, %7, %10, %1\n\tv_dot2_f32_bf16 %2, %8, %10, %2\n\tv_dot2_f32_bf16 %3, %9, %10, %3\n\t"
          "v_mul_f32 %0, %0, %12\n\tv_mul_f32 %1, %1, %13\n\tv_mul_f32 %2, %2, %14\n\tv_mul_f32 %3, %3, %15"
          : "=&v"(ks[0]), "=&v"(ks[1]), "=&v"(ks[2]), "=&v"(ks[3])
          : "v"(P[0]), "v"(P[1]), "v"(P[2]), "v"(P[3]), "v"(P[4]), "v"(P[5]), "v"(wa), "v"(wb), "v"(vf[0]), "v"(vf[1]), "v"(vf[2]), "v"(vf[3]));
      lds_st<v2u>(tw + c * 64, (v2u){pk2(ks[3], ks[2]), pk2(ks[1], ks[0])});
    }
    MEMFENCE();
    {
      v4u d[4];
#pragma unroll
      for (int i = 0; i < 4; ++i) d[i] = lds_ld<v4u>(tr_ + i * 1056);
#pragma unroll
      for (int i = 0; i < 4; ++i) st16<SC1>(d[i], rws, ko + i * 1024);
      SCHED();
      st4<SC1>(chk_make<false>(d, kc, lane) + tag * TAGK, rws, fo);
    }
    MEMFENCE();
    lds_stv<u32>(C_PUB + w * 4, now() | 1u);
    if (w == 0) park();
    lds_wait_nz(C_HC + w * 4);
    cv.stream();
    {
      const u32 ra3 = cv.tl[0].x, yo3 = cv.tl[0].y, co3 = cv.tl[0].z;
      const v4u kq = cv.tl[1];
      lds_wait_min4(C_OUT, 3);
      const v2u o0 = __builtin_bit_cast(v2u, __builtin_amdgcn_ds_read_tr16_b64_v4i16((LAS s16x4*)(size_t)ra3));
      const v2u o1 = __builtin_bit_cast(v2u, __builtin_amdgcn_ds_read_tr16_b64_v4i16((LAS s16x4*)(size_t)(ra3 + 8)));
      const v4u d = {o0.x, o0.y, o1.x, o1.y};
      st16<SC1>(d, rws, yo3);
      u32 h = chk_piece(d, kq, 0u);
      h += dppu<0xB1, 0xf>(h); h += dppu<0x4E, 0xf>(h); h += dppu<0x141, 0xf>(h);
      st4<SC1>(h + tag * TAGK, rws, co3);
      SCHED();
    }
    EpiPrep e;
    e.kc = cv.tl[1]; e.a = cv.tl[2]; e.b = cv.tl[3];
    epilogue<false>(rws, mkrs(p.out, S * D * 2), e, row0, lane);
    ENDW();
  }

  v4u H[4];
  v2u hn[4];
    hold(t0, T_H);
#pragma unroll
    for (int i = 0; i < 4; ++i) H[i] = ld16<NT>(mkrs(p.ssmk, S * D * 2), ch * 4096 + (i * 64 + lane) * 16);
    SCHED();
  {
    const u32 lrow = 2 * jw + (lane >> 5);
    __builtin_amdgcn_s_setprio(LNPRIO);
    float sx = 0.f, sq = 0.f, sr = 0.f, sw = 0.f;
#pragma unroll
    for (int i = 0; i < 16; ++i) { const u32 wv = Q[4 + (i >> 2)][i & 3]; sw = dot2(wv, 0x3f803f80u, sw); }
    asm volatile("" : "+v"(sw) : "v"(codepf));
    SCHED();
    {
      const u32 late = now() - t0;
      if (late > ADAPT) t0 += late - ADAPT;
      if (jw == 1) lds_stv<u32>(C_T0, t0 | 1u);
    }
#pragma unroll
    for (int i = 0; i < 16; ++i) {
      const u32 a = Q[i >> 2][i & 3], wv = Q[4 + (i >> 2)][i & 3];
      sx = dot2(a, 0x3f803f80u, sx); sq = dot2(a, a, sq); sr = dot2(a, wv, sr);
    }
#define RED4(ctl) "v_add_f32_dpp %0, %0, %0 " ctl "\n\tv_add_f32_dpp %1, %1, %1 " ctl "\n\tv_add_f32_dpp %2, %2, %2 " ctl "\n\tv_add_f32_dpp %3, %3, %3 " ctl "\n\t"
    asm("s_nop 2\n\t" RED4("row_shr:1 row_mask:0xf bank_mask:0xf bound_ctrl:1") RED4("row_shr:2 row_mask:0xf bank_mask:0xf bound_ctrl:1")
        RED4("row_shr:4 row_mask:0xf bank_mask:0xf bound_ctrl:1") RED4("row_shr:8 row_mask:0xf bank_mask:0xf bound_ctrl:1")
        RED4("row_bcast:15 row_mask:0xa bank_mask:0xf") : "+v"(sx), "+v"(sq), "+v"(sr), "+v"(sw));
#undef RED4
    const float mu = sx * (1.f / D);
    const u32 mv = pk2(mu, __builtin_fmaxf(__builtin_fmaf(-mu, mu, sq * (1.f / D)), 0.f));
    const float sd = rbf(__builtin_amdgcn_sqrtf(rbf(hi2f(mv) + 1e-5f)));
    const float rs = __builtin_amdgcn_rcpf(sd);
    const u32 rp = pk2(0.f, __builtin_fmaf(-lo2f(mv), sw, sr) * rs);
    const u32 key = ((rp ^ ((u32)((int)rp >> 31) | 0x80000000u)) & 0xFFFF0000u) | (2047u - (row0 + lrow));
    const u32 trow = (row0 & 8) + lrow;
#pragma unroll
    for (int i = 0; i < 2; ++i) {
      lds_st<v4u>(L_XT + trow * XT_P + (lane & 31) * 16 + i * 512, Q[i]);
      lds_st<v4u>(L_XT + (trow ^ 8) * XT_P + (lane & 31) * 16 + i * 512, Q[8 + i]);
    }
    if ((lane & 31) == 31) {
      lds_st<f32x2>(L_RN + trow * 8, (f32x2){rs, -lo2f(mv) * rs});
      lds_st<u32>(L_LN + jw * 16 + (lane >> 5) * 4, (mv & 0xFFFFu) | asu(sd));
      lds_st<u32>(L_LN + jw * 16 + 8 + (lane >> 5) * 4, key);
      MEMFENCE();
      lds_stv<u32>(C_LN + jw * 4, 1u);
    }
    MEMFENCE();
    SCHED();
  }
  {
    v4u rec = {0u, 0u, 0u, 0u};
    u32 ro_ = OOB;
    if (jw == 3) {
      const u32 tag = epoch + 1u;
      lds_wait_nz(C_CLR);
      lds_wait_min4(C_LN, 0);
      const v4u r4 = lds_ld<v4u>(L_LN + (lane & 3) * 16);
      u32 a1 = umax(r4.z, r4.w), a2 = umin(r4.z, r4.w);
      u32 b1 = dppu<0xB1, 0xf>(a1), b2 = dppu<0xB1, 0xf>(a2);
      a2 = umax(umin(a1, b1), umax(a2, b2)); a1 = umax(a1, b1);
      b1 = dppu<0x4E, 0xf>(a1); b2 = dppu<0x4E, 0xf>(a2);
      a2 = umax(umin(a1, b1), umax(a2, b2)); a1 = umax(a1, b1);
      const u32 f0 = lane >= 8 ? a1 : r4.x, f1 = lane >= 8 ? a2 : r4.y;
      rec = (v4u){f0, f1, 0u, (f0 + f1) ^ (tag * TAGK)};
      ro_ = lane < 8 ? WS_REC + (row0 >> 3) * 128 + lane * 16 : lane < 8 + KSTR / 16 ? WS_KEY + L * KSTR + (lane - 8) * 16 : OOB;
    }
    asm volatile("" : "+v"(rec), "+v"(ro_));
    st16<SC1>(rec, rws, ro_);
    SCHED();
    __builtin_amdgcn_s_setprio(0);
  }
  {
    SCHED();
    const u32 hc = L_HC + jw * HC_CH;
#pragma unroll
    for (int i = 0; i < 4; ++i) {
      const u32 nx = i < 3 ? rfl(H[i < 3 ? 1 + i : 3].x) : 0u, ny = i < 3 ? rfl(H[i < 3 ? 1 + i : 3].y) : 0u;
      hn[i].x = __builtin_amdgcn_update_dpp(nx, H[i].x, 0x130, 0xf, 0xf, false);
      hn[i].y = __builtin_amdgcn_update_dpp(ny, H[i].y, 0x130, 0xf, 0xf, false);
    }
#pragma unroll
    for (int i = 0; i < 4; ++i) {
      const v4u d = H[i];
      const u32 e0 = __builtin_amdgcn_alignbit(d.y, d.x, 16), e1 = __builtin_amdgcn_alignbit(d.z, d.y, 16),
                e2 = __builtin_amdgcn_alignbit(d.w, d.z, 16), e3 = __builtin_amdgcn_alignbit(hn[i].x, d.w, 16),
                e4 = __builtin_amdgcn_alignbit(hn[i].y, hn[i].x, 16);
      const u32 a = hc + 32 + (i * 64 + lane) * 16;
      lds_st<v4u>(a, d);
      lds_st<v4u>(a + HC_COPY, (v4u){e0, e1, e2, e3});
      lds_st<v4u>(a + 2 * HC_COPY, (v4u){d.y, d.z, d.w, hn[i].x});
      lds_st<v4u>(a + 3 * HC_COPY, (v4u){e1, e2, e3, e4});
    }
    {
      const u32 s0 = rfl(H[0].x), s1 = rfl(H[0].y);
      const int cb = lane & 3;
      const u32 w3 = cb == 0 ? 0u : cb == 1 ? (s0 << 16) : cb == 2 ? s0 : ((s0 >> 16) | (s1 << 16));
      const v4u z = {0, 0, lane == 7 ? (s0 << 16) : 0u, (lane & 4) ? w3 : 0u};
      if (lane < 8) lds_st<v4u>(hc + cb * HC_COPY + (lane >> 2) * 16, z);
    }
    MEMFENCE();
    lds_stv<u32>(C_HC + jw * 4, 1u);
    SCHED();
  }
  if (jw < 2) {
    v4u R[48];
    const int q4 = lane >> 4, N = lane & 15;
    const rsrc_t rw = mkrs(jw == 0 ? p.wg : p.wu, D * F * 2);
    const u32 wo = ((j & 3) * 256 + (lane >> 3) * 8) * (F * 2) + (j >> 2) * 128 + (lane & 7) * 16;
    if (ffn) {
    const rsrc_t rd = mkrs(p.wd, D * F * 2);
    const u32 dofs = ((j >> 2) * 64 + q4 * 8) * 2048 + (j & 3) * 512 + jw * 256 + N * 16;
    const u32 ha = L_HA + jw * 256;
    u32 ah[8];
#pragma unroll
    for (int c = 0; c < 8; ++c) {
      ah[c] = (N >> 1) == c ? ha + (N & 1) * 128 + q4 * 16 : C_ZERO;
      asm volatile("" : "+v"(ah[c]));
    }
    u32 o2 = WS_DN + j * 2048 + jw * 1024 + lane * 16;
    u32 po = WS_GU + (j & ~3) * 1024 + lane * 8;
    u32 hw = ha + (((lane >> 1) & 7) * 8 + ((lane >> 4) & 1) * 4 + (lane >> 5) * 2 + (lane & 1)) * 2;
    asm volatile("" : "+v"(o2), "+v"(po), "+v"(hw));
#pragma unroll
    for (int i = 0; i < 32; ++i) {
      if (i >= WIN) VMCNT(WIN - 1);
      if ((i & 1) == 0) hold(t0, T_W0 + i * T_WS);
      R[i] = ld16<NT>(rw, wo + ((i >> 3) * 64 + (i & 7)) * (F * 2));
      SCHED();
      if (i >= WIN + 8 && ((i - WIN) & 7) == 0) { perm8(&R[i - WIN - 8]); SCHED(); }
    }
#pragma unroll
    for (int i = 32; i < 32 + DNE; ++i) R[i] = ld16<NT>(rd, dofs + (((i >> 3) & 1) * 32 + (i & 7)) * 2048);
    SCHED();
#pragma unroll
    for (int bk = (31 - WIN) >> 3; bk < 3; ++bk) perm8(&R[bk * 8]);
#pragma unroll
    for (int i = 0; i < 24; ++i) asm volatile("" : "+v"(R[i]));
    const bool kap = (lane >> 3) & 1;
    u32 oo = lane < 32 ? WS_GU + j * 1024 + jw * 512 + lane * 16 : OOB;
    asm volatile("" : "+v"(oo));
    lds_wait_nz(C_XS);
    __builtin_amdgcn_s_setprio(2);
    {
      f32x4 acc[8];
      v4u X[4];
#pragma unroll
      for (int bk = 0; bk < 4; ++bk) X[bk] = lds_ld<v4u>(L_XS + ((lane >> 1) & 1) * 512 + q4 * 32 + (lane & 1) * 16 + bk * 128);
#pragma unroll
      for (int c = 0; c < 8; ++c) acc[c] = (f32x4){0, 0, 0, 0};
#pragma unroll
      for (int bk = 0; bk < 3; ++bk)
#pragma unroll
        for (int c = 0; c < 8; ++c) acc[c] = mfma(X[bk], R[bk * 8 + c], acc[c]);
#pragma unroll
      for (int c = 0; c < 8; ++c) asm volatile("" : "+v"(acc[c]));
      VMCNT(DNE);
#pragma unroll
      for (int i = 32 + DNE; i < 48; ++i) R[i] = ld16<NT>(rd, dofs + (((i >> 3) & 1) * 32 + (i & 7)) * 2048);
      SCHED();
      perm8(&R[24]);
#pragma unroll
      for (int c = 0; c < 8; ++c) acc[c] = mfma(X[3], R[24 + c], acc[c]);
      u32 o[8];
#pragma unroll
      for (int cc = 0; cc < 4; ++cc)
#pragma unroll
        for (int t = 0; t < 2; ++t) {
          const float keep = kap ? acc[4 + cc][2 * t + 1] : acc[cc][2 * t], send = kap ? acc[cc][2 * t + 1] : acc[4 + cc][2 * t];
          o[cc * 2 + t] = nosent(keep + dppf<0x128, 0xf>(send));
        }
      const bool hi = lane & 16;
      st16<SC1>((v4u){hi ? o[4] : o[0], hi ? o[5] : o[1], hi ? o[6] : o[2], hi ? o[7] : o[3]}, rws, oo);
    }
    SCHED();
    const u32 tpub = now();
    SCHED();
    perm8(&R[32]);
#pragma unroll
    for (int i = 32; i < 40; ++i) asm volatile("" : "+v"(R[i]));
    hold(tpub, T_2A);
    v2u pv[8];
    MEMFENCE();
#pragma unroll
    for (int i = 0; i < 8; ++i) pv[i] = ld8<SC1>(rws, po + i * 512);
    SCHED();
    perm8(&R[40]);
#pragma unroll
    for (int i = 40; i < 48; ++i) asm volatile("" : "+v"(R[i]));
    SPIN(SPIN_G) {
      u32 mx = 0;
#pragma unroll
      for (int i = 0; i < 8; ++i) mx = umax(mx, umax(pv[i].x, pv[i].y));
      if (wave_all(mx != SENT)) break;
      MEMFENCE();
#pragma unroll
      for (int i = 0; i < 8; ++i) if (umax(pv[i].x, pv[i].y) == SENT) pv[i] = ld8<SC1>(rws, po + i * 512);
    }
    const float g0 = add1(add1(asf(pv[0].x), asf(pv[2].x)), add1(asf(pv[4].x), asf(pv[6].x))), g1 = add1(add1(asf(pv[0].y), asf(pv[2].y)), add1(asf(pv[4].y), asf(pv[6].y)));
    const float u0 = add1(add1(asf(pv[1].x), asf(pv[3].x)), add1(asf(pv[5].x), asf(pv[7].x))), u1 = add1(add1(asf(pv[1].y), asf(pv[3].y)), add1(asf(pv[5].y), asf(pv[7].y)));
    const u32 gp = pk2(g0, g1), up = pk2(u0, u1);
    const float ga = lo2f(gp), gb = hi2f(gp);
    float ra_ = __builtin_amdgcn_rcpf(1.f + __expf(-ga)), rb_ = __builtin_amdgcn_rcpf(1.f + __expf(-gb));
    asm("s_nop 0" : "+v"(ra_), "+v"(rb_));
    const u32 sp = pk2(mul1(ga, ra_), mul1(gb, rb_));
    const u32 hp = pk2(mul1(lo2f(sp), lo2f(up)), mul1(hi2f(sp), hi2f(up)));
    lds_st<u16>(hw, (u16)hp);
    lds_st<u16>(hw + 128, (u16)(hp >> 16));
    MEMFENCE();
    f32x4 acc = {0, 0, 0, 0};
    v4u Hb[16];
#pragma unroll
    for (int i = 0; i < 16; ++i) Hb[i] = lds_ld<v4u>(ah[i & 7] + (i >> 3) * 64);
#pragma unroll
    for (int i = 0; i < 16; ++i) acc = mfma(R[32 + i], Hb[i], acc);
    st16<SC1>((v4u){nosent(acc[0]), nosent(acc[1]), nosent(acc[2]), nosent(acc[3])}, rws, o2);
    } else {
      hold(t0, T_NF);
    }
    const u32 tdn = now();
    if (jw == 1) {
      __builtin_amdgcn_s_setprio(0);
      EpiPrep e;
      e.make(row0, get_tag(), lane);
      lds_wait_nz(C_YOK);
      epilogue<true>(rws, mkrs(p.out, S * D * 2), e, row0, lane);
      ENDW();
    }
    lds_wait_nz(C_SEL);
    const v4u ks4 = lds_ldv<v4u>(C_SEL);
    const u32 kk = (lane & 1) ? ks4.z : ks4.y;
    const u32 frow = 2047u - (kk & 0x7FFu), kx = kk >> 16;
    const float rsel = lo2f((kx & 0x8000u) ? (kx ^ 0x8000u) : (kx ^ 0xFFFFu));
    float sig = rbf(__builtin_amdgcn_rcpf(1.f + __expf(-rsel)));
    const int fc = (lane >> 1) & 3;
    const float fx = lds_ld<float>(L_FR + (lane & 1) * 32 + fc * 4), fn = lds_ld<float>(L_FR + (lane & 1) * 32 + 16 + fc * 4);
    u32 pa = WS_DN + ((lane >> 3) * 4 + (L >> 6)) * 2048 + ((L >> 5) & 1) * 1024 + ((L >> 3) & 3) * 256 + (L & 1) * 128 + (lane & 7) * 16 + ((L >> 1) & 3) * 4;
    asm volatile("" : "+v"(pa), "+v"(sig));
    lds_wait_min4(C_OUT, (2047u - (umin(rfl(ks4.y) & 0x7FFu, rfl(ks4.z) & 0x7FFu))) >> 9);
    float ob = __builtin_fmaf(fn, lo2f(lds_ld<u16>(L_OS + fc * OS_CH + frow * 2)), fx);
    u32 fo_ = lane < 8 ? frow * 2048 + L * 8 + fc * 2 : OOB;
    asm volatile("" : "+v"(fo_), "+v"(ob));
    hold(tdn, T_2B);
    u32 e[7];
    MEMFENCE();
#pragma unroll
    for (int i = 0; i < 7; ++i) e[i] = __builtin_amdgcn_raw_buffer_load_b32(rws, pa, i * 65536, SC1);
    SPIN(SPIN_G) {
      u32 mx = 0;
#pragma unroll
      for (int i = 0; i < 7; ++i) mx = umax(mx, e[i]);
      if (wave_all(mx != SENT)) break;
      __builtin_amdgcn_s_sleep(1);
      MEMFENCE();
#pragma unroll
      for (int i = 0; i < 7; ++i) if (e[i] == SENT) e[i] = __builtin_amdgcn_raw_buffer_load_b32(rws, pa, i * 65536, SC1);
    }
    float y = add1(add1(add1(asf(e[0]), asf(e[1])), add1(asf(e[2]), asf(e[3]))), add1(add1(asf(e[4]), asf(e[5])), asf(e[6])));
    y += dppf<0x128, 0xf>(y);
    const auto r16 = __builtin_amdgcn_permlane16_swap(asu(y), asu(y), false, false);
    y = asf(r16[0]) + asf(r16[1]);
    const auto r32 = __builtin_amdgcn_permlane32_swap(asu(y), asu(y), false, false);
    __builtin_amdgcn_raw_buffer_store_b16((u16)pk2(__builtin_fmaf(asf(r32[0]) + asf(r32[1]), sig, ob), 0.f), mkrs(p.out, S * D * 2), fo_, 0, 0);
    ENDW();
  }
  if (jw == 2) {
    __builtin_amdgcn_s_setprio(2);
    const u32 tag = get_tag();
    EpiPrep e;
    e.make(row0, tag, lane);
    {
      u32 aI = L_UB + ((lane >> 2) & 3) * UB_CH + 512 + (lane >> 4) * 64 + (lane & 3) * 16;
      u32 io = WS_KX + (cg * 64 + (lane >> 4)) * 4096 + rb * 256 + (lane & 15) * 16;
      u32 co = (lane & 12) ? OOB : WS_KVF + (cg * 64 + (lane & 3) * 4 + (lane >> 4)) * 256 + ((rb & 3) * 16 + (rb >> 2)) * 4;
      const v4u kc = chk_consts(lane);
      u32 tm = (lane & 12) ? 0u : tag * TAGK;
      asm volatile("" : "+v"(aI), "+v"(io), "+v"(co), "+v"(tm));
      v4u d[4][4];
      u32 c[4];
#define LOADS(s_) do { \
        _Pragma("unroll") for (int i = 0; i < 4; ++i) d[s_][i] = __builtin_amdgcn_raw_buffer_load_b128(rws, io, (s_ * 16 + i * 4) * 4096, SC1); \
        c[s_] = __builtin_amdgcn_raw_buffer_load_b32(rws, co, s_ * 4096, SC1); } while (0)
#define ISSUE(s_) do { \
        lds_wait_nz(C_PUB + s_ * 4); \
        hold(rfl(lds_ldv<u32>(C_PUB + s_ * 4)), s_ ? T_KV1 : T_KVP); \
        MEMFENCE(); \
        LOADS(s_); \
        SCHED(); } while (0)
#define LAND(s_) do { \
        SPIN(SPIN_G) { \
          _Pragma("unroll") for (int i = 0; i < 4; ++i) lds_st<v4u>(aI + s_ * 1024 + i * 256, d[s_][i]); \
          const bool bad = chk_bad<false>(d[s_], kc, tm - c[s_]); \
          if (__builtin_amdgcn_ballot_w64(bad) == 0ull) break; \
          MEMFENCE(); \
          if (bad) LOADS(s_); \
          VMCNT(0); \
        } \
        MEMFENCE(); \
        lds_stv<u32>(C_STG + s_ * 4, 1u); \
        SCHED(); } while (0)

      ISSUE(0); LAND(0); ISSUE(1); ISSUE(2); LAND(1); ISSUE(3); LAND(2); LAND(3);
#undef LOADS
#undef ISSUE
#undef LAND
    }
    lds_wait_nz(C_SEL);
  {
    const u32 st = row0 >> 9;
    if (st < 3) { SPIN(SPIN_L) { if (rfl(lds_ldv<u32>(C_YP)) > st) break; __builtin_amdgcn_s_sleep(2); } }
    else lds_wait_min4(C_OUT, 3);
    hold(now(), st == 3 ? T_Y3 : st == 2 ? T_Y : T_Y0);
    lds_stv<u32>(C_YOK, 1u);
  }
  __builtin_amdgcn_s_setprio(0);
  epilogue<true>(rws, mkrs(p.out, S * D * 2), e, row0, lane);
    ENDW();
  }

  const u32 tag = epoch + 1u;
  __builtin_amdgcn_s_setprio(2);
  u32 ra = L_OS + ((lane >> 2) & 3) * OS_CH + ((lane >> 4) * 32 + (lane & 3) * 8) * 2;
  u32 yo = WS_YX + (lane >> 3) * YSTR + L * 128 + (lane & 7) * 16;
  u32 co = WS_YC + L * 2048 + lane * 4;
  const v4u kc = chk_consts(lane & 7);
  u32 xo = (j & 3) * 512 + (lane & 31) * 16, po_ = WS_REC + ((row0 >> 3) ^ 1) * 128 + ((lane >> 1) & 3) * 16;
  u32 eo = (L == 0 && lane == 0) ? WS_EPOCH : OOB, xsa = L_XS + lane * 16, rna = L_RN + (((row0 & 8) ^ 8) + (lane & 7)) * 8;
  asm volatile("" : "+v"(ra), "+v"(yo), "+v"(co), "+v"(xo), "+v"(po_), "+v"(eo), "+v"(xsa), "+v"(rna));
  u32 k1 = 0, k2 = 0;
  {
    u32 go[4];
#pragma unroll
    for (int i = 0; i < 4; ++i) { go[i] = WS_KEY + (i * 64 + lane) * KSTR; asm volatile("" : "+v"(go[i])); }
    hold(t0, T_REC);
    SPIN(SPIN_G) {
      MEMFENCE();
      v4u gr[4];
#pragma unroll
      for (int i = 0; i < 4; ++i) gr[i] = ld16<SC1>(rws, go[i]);
      u32 pend = OOB;
#pragma unroll
      for (int i = 0; i < 4; ++i) {
        const bool ok = (((gr[i].x + gr[i].y + gr[i].z) ^ gr[i].w) == tag * TAGK) & (go[i] != OOB);
        const u32 ka_ = ok ? gr[i].x : 0u, kb_ = ok ? gr[i].y : 0u;
        k2 = umax(med3u(k1, k2, ka_), kb_); k1 = umax(k1, ka_);
        go[i] = ok ? OOB : go[i];
        pend = umin(pend, go[i]);
      }
      if (wave_all(pend == OOB)) break;
      __builtin_amdgcn_s_sleep(SLP_REC);
    }
    MEMFENCE();
  }
  const u32 t1 = wave_max(k1);
  const u32 t2 = wave_max(k1 == t1 ? k2 : k1);
  {
    const u32 myrow = 2047u - (((lane >> 5) ? t2 : t1) & 0x7FFu);
    const u32 so = WS_REC + (myrow >> 3) * 128 + ((myrow >> 1) & 3) * 16;
    const v4u xs = ld16<0>(rq, myrow * 2048 + xo);
    v4u sg = ld16<SC1>(rws, so);
    v4u pg_ = ld16<SC1>(rws, po_);
    const v2u xf = ld8<0>(rq, myrow * 2048 + L * 8);
    st4<SC1>(tag, rws, eo);
    SCHED();
    SPIN(SPIN_G) {
      if (wave_all((((sg.x + sg.y + sg.z) ^ sg.w) == tag * TAGK) & (((pg_.x + pg_.y + pg_.z) ^ pg_.w) == tag * TAGK))) break;
      MEMFENCE();
      sg = ld16<SC1>(rws, so);
      pg_ = ld16<SC1>(rws, po_);
      VMCNT(0);
    }
    const u32 ms = (myrow & 1) ? sg.y : sg.x;
    const float rs = __builtin_amdgcn_rcpf(hi2f(ms)), nb = -lo2f(ms) * rs;
    v4u xn;
#pragma unroll
    for (int i = 0; i < 4; ++i) { const u32 xx = xs[i]; xn[i] = pk2(fma1(lo2f(xx), rs, nb), fma1(hi2f(xx), rs, nb)); }
    lds_st<v4u>(xsa, xn);
    MEMFENCE();
    lds_stv<u32>(C_XS, 1u);
    MEMFENCE();
    SCHED();
    const f32x4 fx = {lo2f(xf.x), hi2f(xf.x), lo2f(xf.y), hi2f(xf.y)};
    if ((lane & 31) == 0) {
      lds_st<f32x4>(L_FR + (lane >> 5) * 32, fx);
      lds_st<f32x4>(L_FR + (lane >> 5) * 32 + 16, (f32x4){fma1(fx[0], rs, nb), fma1(fx[1], rs, nb), fma1(fx[2], rs, nb), fma1(fx[3], rs, nb)});
    }
    const u32 pm = (lane & 1) ? pg_.y : pg_.x;
    const float prs = __builtin_amdgcn_rcpf(hi2f(pm));
    lds_st<f32x2>(rna, (f32x2){prs, -lo2f(pm) * prs});
    MEMFENCE();
    lds_stv<v4u>(C_SEL, (v4u){1u, t1, t2, 0u});
    MEMFENCE();
  }
    {
      hold(t0, T_YP);
#pragma unroll 1
      for (u32 c = 0; c < 3; ++c) {
        lds_wait_min4(C_OUT, c);
        v4u d[4];
#pragma unroll
        for (int i = 0; i < 4; ++i) {
          const v2u o0 = __builtin_bit_cast(v2u, __builtin_amdgcn_ds_read_tr16_b64_v4i16((LAS s16x4*)(size_t)(ra + i * 256)));
          const v2u o1 = __builtin_bit_cast(v2u, __builtin_amdgcn_ds_read_tr16_b64_v4i16((LAS s16x4*)(size_t)(ra + i * 256 + 8)));
          d[i] = (v4u){o0.x, o0.y, o1.x, o1.y};
        }
#pragma unroll
        for (int i = 0; i < 4; ++i) __builtin_amdgcn_raw_buffer_store_b128(d[i], rws, yo, (c * 32 + i * 8) * YSTR, SC1);
        SCHED();
        st4<SC1>(chk_make<true>(d, kc, lane) + tag * TAGK, rws, co);
        lds_stv<u32>(C_YP, c + 1);
        ra += 1024; co += 256;
      }
    }
}

extern "C" void kernel_launch(void* const* d_in, const int* in_sizes, int n_in, void* d_out, int out_size, void* d_ws,
                              size_t ws_size, hipStream_t stream) {
  Params p;
  p.q = (const u16*)d_in[0]; p.k = (const u16*)d_in[1]; p.v = (const u16*)d_in[2];
  p.shk = (const u16*)d_in[3]; p.ssmk = (const u16*)d_in[4]; p.ssmD = (const u16*)d_in[5];
  p.wr = (const u16*)d_in[7]; p.wg = (const u16*)d_in[8]; p.wu = (const u16*)d_in[9]; p.wd = (const u16*)d_in[10];
  p.out = (u16*)d_out; p.ws = (char*)d_ws;
  (void)hipFuncSetAttribute((const void*)mega, hipFuncAttributeMaxDynamicSharedMemorySize, L_END);
  hipLaunchKernelGGL(mega, dim3(256), dim3(512), L_END, stream, p);
}
```
